```python
import torch

def _softmax(x, axis=-1):
    return torch.softmax(x, dim=axis)

def _gelu(x):
    return torch.nn.functional.gelu(x, approximate='tanh')

def _silu(x):
    return torch.nn.functional.silu(x)

def _sigmoid(x):
    return torch.sigmoid(x)

def _layernorm(x, axis=-1, eps=1e-05):
    m = x.mean(dim=axis, keepdim=True)
    v = x.var(dim=axis, keepdim=True, unbiased=False)
    return (x - m) / torch.sqrt(v + eps)

def _scatter_add(tgt, idx, src, axis=0):
    return tgt.clone().index_add_(axis, idx, src)

def _segment_sum(src, seg, n):
    out = torch.zeros((n,) + src.shape[1:], dtype=src.dtype)
    return out.index_add_(0, seg, src)

def _pad(x, pw):
    flat = tuple((v for pair in reversed(pw) for v in pair))
    return torch.nn.functional.pad(x, flat)
kernel_name = 'recurrent_scan__memory_aug_recurrent_mem_tr__glu_gated_geglu__prompt'

def setup_inputs():

    _w = lambda a: (lambda t: t.to(torch.bfloat16) if t.is_floating_point() else t)(torch.as_tensor(a))

    def _A_setup():
        B, S, Dm = (1, 2048, 512)
        H, Dh = (32, 128)
        M, seg = (8, 32)
        seg = min(seg, S)
        S = S // seg * seg
        return {'x': _w((lambda *s: torch.randn(*s, dtype=torch.bfloat16) * 0.1)(B, S, Dm)), 'mem0': _w((lambda *s: torch.randn(*s, dtype=torch.bfloat16) * 1.6)(B, M, Dm)), 'Wq': _w((lambda *s: torch.randn(*s, dtype=torch.bfloat16) * 0.8)(Dm, H, Dh) * Dm ** (-0.5)), 'Wk': _w((lambda *s: torch.randn(*s, dtype=torch.bfloat16) * 0.8)(Dm, H, Dh) * Dm ** (-0.5)), 'Wv': _w((lambda *s: torch.randn(*s, dtype=torch.bfloat16) * 1.6)(Dm, H, Dh) * Dm ** (-0.5)), 'Wo': _w((lambda *s: torch.randn(*s, dtype=torch.bfloat16) * 1.6)(H, Dh, Dm) * (H * Dh) ** (-0.5)), 'seg_len': seg}
    _A_in = _A_setup()

    def _B_setup():
        B, S, Dm, Df = (1, 2048, 512, 1792)
        return {'x': _w((lambda *s: torch.randn(*s, dtype=torch.bfloat16) * 0.1)(B, S, Dm)), 'w_gate': _w((lambda *s: torch.randn(*s, dtype=torch.bfloat16) * 0.4)(Dm, Df) * Dm ** (-0.5)), 'w_up': _w((lambda *s: torch.randn(*s, dtype=torch.bfloat16) * 0.4)(Dm, Df) * Dm ** (-0.5)), 'w_down': _w((lambda *s: torch.randn(*s, dtype=torch.bfloat16) * 0.4)(Df, Dm) * Df ** (-0.5))}
    _B_in = _B_setup()
    return {'A_x': _A_in['x'], 'A_mem0': _A_in['mem0'], 'A_Wq': _A_in['Wq'], 'A_Wk': _A_in['Wk'], 'A_Wv': _A_in['Wv'], 'A_Wo': _A_in['Wo'], 'A_seg_len': _A_in['seg_len'], 'B_x': _B_in['x'], 'B_w_gate': _B_in['w_gate'], 'B_w_up': _B_in['w_up'], 'B_w_down': _B_in['w_down']}

def reference(A_x, A_mem0, A_Wq, A_Wk, A_Wv, A_Wo, A_seg_len, B_x, B_w_gate, B_w_up, B_w_down):

    def _A_ref(x, mem0, Wq, Wk, Wv, Wo, seg_len):
        B, S, Dm = x.shape
        H, Dh = (Wq.shape[1], Wq.shape[2])
        M = mem0.shape[1]
        scale = Dh ** (-0.5)
        n_seg = S // seg_len
        L = M + seg_len
        idx = torch.arange(L)
        mask = (idx[:, None] < M) | (idx[:, None] >= idx[None, :])
        mem = mem0
        outs = []
        for s in range(n_seg):
            xs = x[:, s * seg_len:(s + 1) * seg_len, :].clone()
            h = torch.concatenate([mem, xs], axis=1)
            q = torch.einsum('btd,dhe->bhte', h, Wq)
            k = torch.einsum('btd,dhe->bhte', h, Wk)
            v = torch.einsum('btd,dhe->bhte', h, Wv)
            scores = torch.einsum('bhqe,bhke->bhqk', q, k) * scale
            scores = torch.where(mask[None, None], scores, -1000000000.0)
            p = _softmax(scores, axis=-1)
            o = torch.einsum('bhqk,bhke->bhqe', p, v)
            o = torch.einsum('bhte,hed->btd', o, Wo)
            mem = o[:, :M, :].clone()
            outs.append(o[:, M:, :])
        return torch.concatenate(outs, axis=1)

    def _B_ref(x, w_gate, w_up, w_down):
        g = torch.einsum('bsd,df->bsf', x, w_gate)
        u = torch.einsum('bsd,df->bsf', x, w_up)
        h = _gelu(g) * u
        return torch.einsum('bsf,fd->bsd', h, w_down)
    x = A_x
    xn = _layernorm(x, axis=-1)
    s = _A_ref(xn, A_mem0, A_Wq, A_Wk, A_Wv, A_Wo, A_seg_len)
    g = _silu(_B_ref(xn, B_w_gate, B_w_up, B_w_down))
    return x + s * g
```

```cpp
#include <hip/hip_runtime.h>
#include <hip/hip_bf16.h>
typedef unsigned short u16;
__device__ __forceinline__ float b2f(u16 x) { return __uint_as_float((unsigned)x << 16); }
__device__ __forceinline__ u16 f2b(float f) { __hip_bfloat16 b = __float2bfloat16(f); return *(u16*)&b; }
__device__ __forceinline__ float rb(float f) { return b2f(f2b(f)); }

__global__ void ln_k(const u16* x, u16* xn) {
    int r = blockIdx.x, l = threadIdx.x;
    float v[8], s = 0;
    for (int i = 0; i < 8; ++i) { v[i] = b2f(x[r * 512 + l * 8 + i]); s += v[i]; }
    for (int o = 32; o; o >>= 1) s += __shfl_xor(s, o);
    float m = s / 512, q = 0;
    for (int i = 0; i < 8; ++i) q += (v[i] - m) * (v[i] - m);
    for (int o = 32; o; o >>= 1) q += __shfl_xor(q, o);
    float rs = rsqrtf(q / 512 + 1e-5f);
    for (int i = 0; i < 8; ++i) xn[r * 512 + l * 8 + i] = f2b((v[i] - m) * rs);
}
__global__ void gemm_k(const u16* A, const u16* B, u16* C, int M, int N, int K) {
    __shared__ float As[16][33], Bs[32][65];
    int tx = threadIdx.x & 63, ty = threadIdx.x >> 6;
    int n0 = blockIdx.x * 64, m0 = blockIdx.y * 16;
    float acc[4] = {0, 0, 0, 0};
    for (int k0 = 0; k0 < K; k0 += 32) {
        for (int i = threadIdx.x; i < 512; i += 256) { int r = i >> 5, c = i & 31; As[r][c] = (m0 + r < M) ? b2f(A[(size_t)(m0 + r) * K + k0 + c]) : 0.f; }
        for (int i = threadIdx.x; i < 2048; i += 256) { int r = i >> 6, c = i & 63; Bs[r][c] = b2f(B[(size_t)(k0 + r) * N + n0 + c]); }
        __syncthreads();
        for (int k = 0; k < 32; ++k) { float b = Bs[k][tx]; for (int i = 0; i < 4; ++i) acc[i] += As[ty * 4 + i][k] * b; }
        __syncthreads();
    }
    for (int i = 0; i < 4; ++i) if (m0 + ty * 4 + i < M) C[(size_t)(m0 + ty * 4 + i) * N + n0 + tx] = f2b(acc[i]);
}
__global__ void attn_k(const u16* Qp, const u16* Km, const u16* Vm, const u16* Kx, const u16* Vx, u16* O, int causal, int segstride_q, int segstride_m) {
    int h = blockIdx.x, qi = blockIdx.y, s = blockIdx.z, l = threadIdx.x;
    const u16* q = Qp + (size_t)(s * segstride_q + qi) * 4096 + h * 128;
    const u16* km = Km + (size_t)s * segstride_m * 4096 + h * 128; const u16* vm = Vm + (size_t)s * segstride_m * 4096 + h * 128;
    const u16* kx = Kx + (size_t)s * 32 * 4096 + h * 128; const u16* vx = Vx + (size_t)s * 32 * 4096 + h * 128;
    float sc = -3e38f;
    if (l < 40 && (!causal || l < 8 || l - 8 <= qi)) {
        const u16* k = l < 8 ? km + l * 4096 : kx + (l - 8) * 4096;
        float a = 0; for (int e = 0; e < 128; ++e) a += b2f(q[e]) * b2f(k[e]);
        sc = a * 0.08838834764831845f;
    }
    float m = sc; for (int o = 32; o; o >>= 1) m = fmaxf(m, __shfl_xor(m, o));
    float p = sc > -1e38f ? __expf(sc - m) : 0.f, sum = p;
    for (int o = 32; o; o >>= 1) sum += __shfl_xor(sum, o);
    p = rb(p / sum);
    float o0 = 0, o1 = 0;
    for (int j = 0; j < 40; ++j) { float pj = __shfl(p, j); const u16* v = j < 8 ? vm + j * 4096 : vx + (j - 8) * 4096; o0 += pj * b2f(v[l * 2]); o1 += pj * b2f(v[l * 2 + 1]); }
    u16* o = O + (size_t)(s * segstride_q + qi) * 4096 + h * 128;
    o[l * 2] = f2b(o0); o[l * 2 + 1] = f2b(o1);
}
__global__ void geglu_k(const u16* g, const u16* u, u16* h, int n) {
    int i = blockIdx.x * 256 + threadIdx.x; if (i >= n) return;
    float x = b2f(g[i]); float t = tanhf(0.7978845608028654f * (x + 0.044715f * x * x * x));
    h[i] = f2b(rb(0.5f * x * (1.f + t)) * b2f(u[i]));
}
__global__ void fin_k(const u16* x, const u16* s, const u16* f, u16* out, int n) {
    int i = blockIdx.x * 256 + threadIdx.x; if (i >= n) return;
    float y = b2f(f[i]); float g = rb(y / (1.f + __expf(-y)));
    out[i] = f2b(b2f(x[i]) + rb(b2f(s[i]) * g));
}
extern "C" void kernel_launch(void* const* d_in, const int* in_sizes, int n_in, void* d_out, int out_size, void* d_ws, size_t ws_size, hipStream_t stream) {
    const u16 *x = (const u16*)d_in[0], *mem0 = (const u16*)d_in[1], *Wq = (const u16*)d_in[2], *Wk = (const u16*)d_in[3], *Wv = (const u16*)d_in[4], *Wo = (const u16*)d_in[5];
    const u16 *wg = (const u16*)d_in[8], *wu = (const u16*)d_in[9], *wd = (const u16*)d_in[10];
    u16* w = (u16*)d_ws; size_t o = 0;
    auto al = [&](size_t n) { u16* p = w + o; o += n; return p; };
    u16 *xn = al(2048 * 512), *Q = al(2048 * 4096), *K = al(2048 * 4096), *V = al(2048 * 4096), *O = al(2048 * 4096), *G = al(2048 * 1792), *U = al(2048 * 1792), *H = al(2048 * 1792);
    u16 *F = al(2048 * 512), *S = al(2048 * 512), *mem = al(65 * 8 * 512), *Qm = al(8 * 4096), *Km = al(64 * 8 * 4096), *Vm = al(64 * 8 * 4096), *Om = al(8 * 4096);
    ln_k<<<2048, 64, 0, stream>>>(x, xn);
    gemm_k<<<dim3(64, 128), 256, 0, stream>>>(xn, Wq, Q, 2048, 4096, 512);
    gemm_k<<<dim3(64, 128), 256, 0, stream>>>(xn, Wk, K, 2048, 4096, 512);
    gemm_k<<<dim3(64, 128), 256, 0, stream>>>(xn, Wv, V, 2048, 4096, 512);
    gemm_k<<<dim3(28, 128), 256, 0, stream>>>(xn, wg, G, 2048, 1792, 512);
    gemm_k<<<dim3(28, 128), 256, 0, stream>>>(xn, wu, U, 2048, 1792, 512);
    geglu_k<<<2048 * 7, 256, 0, stream>>>(G, U, H, 2048 * 1792);
    gemm_k<<<dim3(8, 128), 256, 0, stream>>>(H, wd, F, 2048, 512, 1792);
    hipMemcpyAsync(mem, mem0, 8 * 512 * 2, hipMemcpyDeviceToDevice, stream);
    for (int s = 0; s < 64; ++s) {
        u16* m = mem + s * 4096;
        gemm_k<<<dim3(64, 1), 256, 0, stream>>>(m, Wk, Km + (size_t)s * 8 * 4096, 8, 4096, 512);
        gemm_k<<<dim3(64, 1), 256, 0, stream>>>(m, Wv, Vm + (size_t)s * 8 * 4096, 8, 4096, 512);
        if (s == 63) break;
        gemm_k<<<dim3(64, 1), 256, 0, stream>>>(m, Wq, Qm, 8, 4096, 512);
        attn_k<<<dim3(32, 8, 1), 64, 0, stream>>>(Qm, Km + (size_t)s * 8 * 4096, Vm + (size_t)s * 8 * 4096, K + (size_t)s * 32 * 4096, V + (size_t)s * 32 * 4096, Om, 0, 0, 0);
        gemm_k<<<dim3(8, 1), 256, 0, stream>>>(Om, Wo, m + 4096, 8, 512, 4096);
    }
    attn_k<<<dim3(32, 32, 64), 64, 0, stream>>>(Q, Km, Vm, K, V, O, 1, 32, 8);
    gemm_k<<<dim3(8, 128), 256, 0, stream>>>(O, Wo, S, 2048, 512, 4096);
    fin_k<<<4096, 256, 0, stream>>>(x, S, F, (u16*)d_out, 2048 * 512);
}
```

```cpp
#include <hip/hip_runtime.h>
#include <hip/hip_bf16.h>

#define GAS __attribute__((address_space(1)))
#define LAS __attribute__((address_space(3)))
#define RLX_AGENT __ATOMIC_RELAXED, __HIP_MEMORY_SCOPE_AGENT
typedef unsigned short u16;
typedef unsigned char u8;
typedef unsigned v4u __attribute__((ext_vector_type(4)));
typedef unsigned v2u __attribute__((ext_vector_type(2)));
typedef float f4 __attribute__((ext_vector_type(4)));
typedef float f2 __attribute__((ext_vector_type(2)));
typedef short s4 __attribute__((ext_vector_type(4)));
typedef __bf16 bf2 __attribute__((ext_vector_type(2)));
typedef __bf16 bf8 __attribute__((ext_vector_type(8)));
typedef __amdgpu_buffer_rsrc_t rsrc_t;

constexpr int L_O = 28672, L_END = 161792;
constexpr unsigned F_TICK = 0, F_RDY = 0x800, F_ACK = 0x900, F_KV = 0x1000, F_KM = 0x4000, F_H = 0x5000, F_X = 0x6000, F_O = 0x8000, F_BYTES = 0x10000;
constexpr unsigned O_XF = 2u << 20, O_KX = 4u << 20, O_VX = 20u << 20, O_QX = 36u << 20, O_OF = 52u << 20, O_HF = 68u << 20, O_KVM = 77u << 20, O_DBG = 86u << 20;
constexpr int Q0 = 72, A0 = 1840, H0 = 50;
constexpr int SPIN_CAP = 1 << 19;

struct Params {
    const u16 *x, *mem0, *Wq, *Wk, *Wv, *Wo, *wg, *wu, *wd;
    u16* out;
    u8 *ws, *win;
    int DA0, DA1, DA2, DA3, DB, DZ, DP, D0, GAP, BQ;
};

__device__ __forceinline__ rsrc_t mkrsrc(const void* p) { return __builtin_amdgcn_make_buffer_rsrc((void*)p, 0, 0x7fffffff, 0x00020000); }
__device__ __forceinline__ v4u ld16(rsrc_t r, unsigned v, unsigned s) { return __builtin_amdgcn_raw_buffer_load_b128(r, v, s, 16); }
__device__ __forceinline__ unsigned ld4(rsrc_t r, unsigned v, unsigned s) { return __builtin_amdgcn_raw_buffer_load_b32(r, v, s, 16); }
__device__ __forceinline__ void st16(v4u d, rsrc_t r, unsigned v, unsigned s) { __builtin_amdgcn_raw_buffer_store_b128(d, r, v + s, 0, 16); }
__device__ __forceinline__ void drain() { asm volatile("s_waitcnt vmcnt(0)" ::: "memory"); }
__device__ __forceinline__ unsigned pk(float a, float b) { return __builtin_bit_cast(unsigned, __builtin_convertvector(f2{a, b}, bf2)); }
__device__ __forceinline__ float rbf(float a) { return __uint_as_float(pk(0.f, a) & 0xffff0000u); }
__device__ __forceinline__ float lo(unsigned v) { return __uint_as_float(v << 16); }
__device__ __forceinline__ float hi(unsigned v) { return __uint_as_float(v & 0xffff0000u); }
__device__ __forceinline__ f4 mma(v4u a, v4u b, f4 c) { return __builtin_amdgcn_mfma_f32_16x16x32_bf16(__builtin_bit_cast(bf8, a), __builtin_bit_cast(bf8, b), c, 0, 0, 0); }
__device__ __forceinline__ f4 mma16(v2u a, v2u b, f4 c) { return __builtin_amdgcn_mfma_f32_16x16x16bf16_1k(__builtin_bit_cast(s4, a), __builtin_bit_cast(s4, b), c, 0, 0, 0); }
template <int NT = 0>
__device__ __forceinline__ void glds(const void* g, unsigned lds_dst) {
    unsigned keep;
    if (NT) asm volatile("s_mov_b32 %0, m0\n\ts_mov_b32 m0, %2\n\ts_nop 0\n\tglobal_load_lds_dwordx4 %1, off nt\n\ts_mov_b32 m0, %0" : "=&s"(keep) : "v"(g), "s"(lds_dst) : "memory");
    else
    asm volatile("s_mov_b32 %0, m0\n\ts_mov_b32 m0, %2\n\ts_nop 0\n\tglobal_load_lds_dwordx4 %1, off\n\ts_mov_b32 m0, %0" : "=&s"(keep) : "v"(g), "s"(lds_dst) : "memory");
}
__device__ __forceinline__ v2u trd(unsigned addr) { return __builtin_bit_cast(v2u, __builtin_amdgcn_ds_read_tr16_b64_v4i16((LAS s4*)(size_t)addr)); }
__device__ __forceinline__ void wait_ge(rsrc_t rf, unsigned off, bool lazy, unsigned need) {
#pragma nounroll
    for (int sp = 0; sp < SPIN_CAP; ++sp) {
        unsigned v = ld4(rf, off, 0);
        if (__all(v >= need)) break;
        if (lazy) __builtin_amdgcn_s_sleep(40); else __builtin_amdgcn_s_sleep(3);
    }
    __builtin_amdgcn_fence(__ATOMIC_ACQUIRE, "wavefront");
}
__device__ __forceinline__ float xor16(float v) { auto r = __builtin_amdgcn_permlane16_swap(__float_as_uint(v), __float_as_uint(v), false, false); return __uint_as_float(r[0] ^ r[1] ^ __float_as_uint(v)); }
__device__ __forceinline__ float xor32(float v) { auto r = __builtin_amdgcn_permlane32_swap(__float_as_uint(v), __float_as_uint(v), false, false); return __uint_as_float(r[0] ^ r[1] ^ __float_as_uint(v)); }
template <int R> __device__ __forceinline__ float ror(float v) { return __int_as_float(__builtin_amdgcn_update_dpp(0, __float_as_int(v), 0x120 + R, 0xf, 0xf, false)); }
__device__ __forceinline__ float rowsum(float v) { v += ror<8>(v); v += ror<4>(v); v += ror<2>(v); v += ror<1>(v); return v; }

__device__ __forceinline__ unsigned tick_read(const Params& p) { return __hip_atomic_load((GAS unsigned*)(p.ws + F_TICK), RLX_AGENT); }
__device__ __forceinline__ void tick_wait(const Params& p, unsigned seen) {
#pragma nounroll
    for (int sp = 0; sp < SPIN_CAP && tick_read(p) == seen; ++sp) __builtin_amdgcn_s_sleep(2);
    __builtin_amdgcn_fence(__ATOMIC_ACQUIRE, "wavefront");
}
__device__ __forceinline__ void tick_ack(const Params& p) { if (threadIdx.x == 0) __hip_atomic_fetch_add((GAS unsigned*)(p.ws + F_ACK + (blockIdx.x & 7) * 128), 1u, RLX_AGENT); }
__device__ __forceinline__ void tick_master(const Params& p, unsigned seen, int w, int l) {
    rsrc_t rf = mkrsrc(p.ws);
    for (unsigned o = F_RDY + threadIdx.x * 128; o < F_O + 256 * 128; o += 256 * 128) { st16(v4u{0u, 0u, 0u, 0u}, rf, o, 0); st16(v4u{0u, 0u, 0u, 0u}, rf, o, 16); }
    drain();
    __syncthreads();
    if (w) return;
#pragma nounroll
    for (int sp = 0; sp < SPIN_CAP; ++sp) {
        if (l == 0) __hip_atomic_store((GAS unsigned*)(p.ws + F_TICK), seen + 1 + sp, RLX_AGENT);
        if (sp == 0) tick_ack(p);
        __builtin_amdgcn_s_sleep(16);
        if ((sp & 3) == 3 && __all(ld4(rf, F_ACK + (l & 7) * 128, 0) == 32)) break;
    }
}
__device__ __forceinline__ void touch(const void* g, unsigned dst = 0x24000) {
    unsigned keep;
    asm volatile("s_mov_b32 %0, m0\n\ts_mov_b32 m0, %2\n\ts_nop 0\n\tglobal_load_lds_dword %1, off\n\ts_mov_b32 m0, %0" : "=&s"(keep) : "v"(g), "s"(dst) : "memory");
}
__device__ __forceinline__ void warm(const Params& p, int i, int w, int l) {
    const unsigned o = (i * 4 + w) * 1024 + l * 16;
#pragma unroll
    for (int m = 3 == 1 ? 0 : 2; m < 5; ++m) {
        const u8* a = (const u8*)(m == 0 ? p.Wk : m == 1 ? p.Wv : m == 3 ? p.Wo : p.Wq) + (m == 2 ? 2 << 20 : 0) + o;
#pragma unroll 4
        for (int k = 0; k < ((m == 2 || m == 4) ? 8 : 16); ++k) glds<0>(a + (size_t)k * 262144, 0x24000);
    }
}
#define STAMP(i) do {} while (0)
__device__ __forceinline__ void ln_tile(const Params& p, bool tick, unsigned seen, int tile, int w, int l) {
    const int tok = w * 4 + (l >> 4), c = l & 15;
    const v4u* src = (const v4u*)(p.x + (size_t)(tile * 16 + tok) * 512);
    v4u v[4];
#pragma unroll
    for (int i = 0; i < 4; ++i) v[i] = src[c + 16 * i];
    float f[32], s = 0.f;
#pragma unroll
    for (int i = 0; i < 16; ++i) { f[2 * i] = lo(v[i >> 2][i & 3]); f[2 * i + 1] = hi(v[i >> 2][i & 3]); s += f[2 * i] + f[2 * i + 1]; }
    const float m = rowsum(s) * (1.f / 512.f);
    float q = 0.f;
#pragma unroll
    for (int i = 0; i < 32; ++i) { f[i] -= m; q += f[i] * f[i]; }
    const float rs = 1.f / sqrtf(rowsum(q) * (1.f / 512.f) + 1e-5f);
    rsrc_t rw = mkrsrc(p.win);
#pragma unroll
    for (int i = 0; i < 4; ++i) {
        v4u o;
#pragma unroll
        for (int j = 0; j < 4; ++j) o[j] = pk(f[8 * i + 2 * j] * rs, f[8 * i + 2 * j + 1] * rs);
        st16(o, rw, ((c >> 2) + 4 * i) * 1024 + ((c & 3) * 16 + tok) * 16, O_XF + tile * 16384);
    }
    if (tick && w == 0) tick_wait(p, seen);
    drain();
    __syncthreads();
    STAMP(1);
    if (threadIdx.x == 0) __hip_atomic_store((GAS unsigned*)(p.ws + F_X + (tile >> 1) * 128 + (tile & 1) * 4), 1u, RLX_AGENT);
    if (tick) tick_ack(p);
}

__device__ __forceinline__ void ffn_role(const Params& p, int id, int rot, int s0, int s1, int w, int l) {
    const int n = l & 15, g = l >> 4, par = w >> 1, cs = w & 1;
    {
        const u8 *b0 = (const u8*)(p.wg + id * 64), *b1 = (const u8*)(p.wu + id * 64) - 128;
        const u8* a[2];
#pragma unroll
        for (int i = 0; i < 2; ++i) { const int c = n ^ (2 * (g | (i << 2))); a[i] = (c < 8 ? b0 : b1) + (size_t)(w * 128 + g) * 3584 + c * 16; }
#pragma unroll 4
        for (int ii = 0; ii < 32; ++ii) { glds(a[(ii >> 1) & 1] + (size_t)ii * 4 * 3584, w * 32768 + ii * 1024); asm volatile("s_waitcnt vmcnt(63)" ::: "memory"); }
        drain();
        __syncthreads();
    }
    v4u W[4][16];
    {
        const int qq = (l >> 2) & 3, pp = l & 3, swz = 2 * (qq | ((g & 1) << 2));
#pragma unroll
        for (int t = 0; t < 4; ++t) {
            const unsigned ad = (8 * g + qq) * 256 + (((t >> 1) * 8 + cs * 4 + (t & 1) * 2 + (pp >> 1)) ^ swz) * 16 + (pp & 1) * 8;
#pragma unroll
            for (int k = 0; k < 16; ++k) { const unsigned ak = ad + ((k + rot) & 15) * 8192; v2u x = trd(ak), y = trd(ak + 1024); W[t][k] = v4u{x[0], x[1], y[0], y[1]}; }
        }
    }
    rsrc_t rw = mkrsrc(p.win), rf = mkrsrc(p.ws);
    GAS unsigned* flag = (GAS unsigned*)(p.ws + F_H + id * 128 + (s0 ? 16 : 0) + w * 4);
    unsigned long long ready = 0;
    auto need = [&](int s) {
#pragma nounroll
        for (int sp = 0; !((ready >> s) & 1) && sp < SPIN_CAP; ++sp) {
            if (sp) __builtin_amdgcn_s_sleep(3);
            ready = __ballot((ld4(rf, F_X + l * 128, 0) & ld4(rf, F_X + l * 128 + 4, 0)) != 0);
        }
        __builtin_amdgcn_fence(__ATOMIC_ACQUIRE, "wavefront");
    };
    v4u X[2][16];
    need(s0 + par);
#pragma unroll
    for (int k = 0; k < 32; ++k) X[k & 1][k >> 1] = ld16(rw, l * 16, O_XF + (s0 + par) * 32768 + (k & 1) * 16384 + (((k >> 1) + rot) & 15) * 1024);
    for (int s = s0 + par; s < s1; s += 2) {
        const int sn = min(s + 2, 62 + par);
        need(sn);
        f4 acc[4][2];
#pragma unroll
        for (int k = 0; k < 16; ++k) {
#pragma unroll
            for (int T = 0; T < 2; ++T) {
                __builtin_amdgcn_sched_barrier(0);
#pragma unroll
                for (int t = 0; t < 4; ++t) acc[t][T] = mma(W[t][k], X[T][k], k ? acc[t][T] : f4{0.f, 0.f, 0.f, 0.f});
                X[T][k] = ld16(rw, l * 16, O_XF + sn * 32768 + T * 16384 + ((k + rot) & 15) * 1024);
            }
        }
        __builtin_amdgcn_sched_barrier(0);
#pragma unroll
        for (int T = 0; T < 2; ++T) {
            float h[8];
#pragma unroll
            for (int i = 0; i < 8; ++i) {
                const float x = rbf(acc[i >> 2][T][i & 3]), uu = rbf(acc[2 + (i >> 2)][T][i & 3]);
                const float e = __builtin_amdgcn_exp2f(-2.302208198144325f * (x + 0.044715f * x * x * x));
                h[i] = rbf(x * __builtin_amdgcn_rcpf(1.f + e)) * uu;
            }
            st16(v4u{pk(h[0], h[1]), pk(h[2], h[3]), pk(h[4], h[5]), pk(h[6], h[7])}, rw, l * 16, O_HF + (id * 2 + cs) * 1024 + (s * 2 + T) * 57344);
        }
        drain();
        if (s == s1 - 2) STAMP(7);
        if (l == 0) __hip_atomic_store(flag, (unsigned)(s + 2), RLX_AGENT);
    }
    __syncthreads();
}

__device__ __forceinline__ void qkv_role(int h, int ty, int tk, int w) {
    asm volatile(
        "s_mov_b32 s34, %[h]\n"
        "s_mov_b32 s35, %[w]\n"
        "s_mov_b32 s36, %[ty]\n"
        "s_mov_b32 s37, %[tk]\n"
        "s_mov_b32 s38, %[ack]\n"
        "s_mov_b64 s[98:99], %[ka]\n"
        "s_lshl_b32 s64, s36, 3\n"
        "s_add_u32 s64, s64, 16\n"
        "s_load_dwordx2 s[60:61], s[98:99], s64\n"
        "s_load_dwordx4 s[20:23], s[98:99], 80\n"
        "v_mbcnt_lo_u32_b32 v17, -1, 0\n"
        "v_mbcnt_hi_u32_b32 v17, -1, v17\n"
        "s_mov_b32 s67, 0\n"
        "s_lshr_b32 s64, s35, 1\n"
        "s_lshl_b32 s64, s64, 3\n"
        "v_bfe_u32 v16, v17, 4, 2\n"
        "v_lshl_add_u32 v14, v16, 1, s64\n"
        "v_and_b32 v15, 15, v17\n"
        "v_xor_b32 v14, v14, v15\n"
        "v_lshlrev_b32 v14, 4, v14\n"
        "v_bfe_u32 v16, v17, 4, 2\n"
        "v_lshl_add_u32 v8, v16, 13, v14\n"
        "s_mul_i32 s66, s34, 256\n"
        "s_mul_i32 s65, s35, 32768\n"
        "s_add_u32 s66, s66, s65\n"
        "s_lshl_b32 m0, s35, 10\n"
        "s_movk_i32 s62, 0x1000\n"
        "s_mov_b32 s63, 0x20000\n"
        "v_mov_b32 v18, 0\n"
        "s_waitcnt lgkmcnt(0)\n"
        "s_add_u32 s60, s60, s66\n"
        "s_addc_u32 s61, s61, 0\n"
        "global_load_dword v24, v18, s[20:21] sc1\n"
        "global_load_lds_dwordx4 v8, s[60:61]\n"
        "s_add_u32 m0, m0, s62\n"
        "s_add_u32 s60, s60, s63\n"
        "s_addc_u32 s61, s61, 0\n"
        "global_load_lds_dwordx4 v8, s[60:61]\n"
        "s_add_u32 m0, m0, s62\n"
        "s_add_u32 s60, s60, s63\n"
        "s_addc_u32 s61, s61, 0\n"
        "global_load_lds_dwordx4 v8, s[60:61]\n"
        "s_add_u32 m0, m0, s62\n"
        "s_add_u32 s60, s60, s63\n"
        "s_addc_u32 s61, s61, 0\n"
        "global_load_lds_dwordx4 v8, s[60:61]\n"
        "s_add_u32 m0, m0, s62\n"
        "s_add_u32 s60, s60, s63\n"
        "s_addc_u32 s61, s61, 0\n"
        "global_load_lds_dwordx4 v8, s[60:61]\n"
        "s_add_u32 m0, m0, s62\n"
        "s_add_u32 s60, s60, s63\n"
        "s_addc_u32 s61, s61, 0\n"
        "global_load_lds_dwordx4 v8, s[60:61]\n"
        "s_add_u32 m0, m0, s62\n"
        "s_add_u32 s60, s60, s63\n"
        "s_addc_u32 s61, s61, 0\n"
        "global_load_lds_dwordx4 v8, s[60:61]\n"
        "s_add_u32 m0, m0, s62\n"
        "s_add_u32 s60, s60, s63\n"
        "s_addc_u32 s61, s61, 0\n"
        "global_load_lds_dwordx4 v8, s[60:61]\n"
        "s_add_u32 m0, m0, s62\n"
        "s_add_u32 s60, s60, s63\n"
        "s_addc_u32 s61, s61, 0\n"
        "global_load_lds_dwordx4 v8, s[60:61]\n"
        "s_add_u32 m0, m0, s62\n"
        "s_add_u32 s60, s60, s63\n"
        "s_addc_u32 s61, s61, 0\n"
        "global_load_lds_dwordx4 v8, s[60:61]\n"
        "s_add_u32 m0, m0, s62\n"
        "s_add_u32 s60, s60, s63\n"
        "s_addc_u32 s61, s61, 0\n"
        "global_load_lds_dwordx4 v8, s[60:61]\n"
        "s_add_u32 m0, m0, s62\n"
        "s_add_u32 s60, s60, s63\n"
        "s_addc_u32 s61, s61, 0\n"
        "global_load_lds_dwordx4 v8, s[60:61]\n"
        "s_add_u32 m0, m0, s62\n"
        "s_add_u32 s60, s60, s63\n"
        "s_addc_u32 s61, s61, 0\n"
        "global_load_lds_dwordx4 v8, s[60:61]\n"
        "s_add_u32 m0, m0, s62\n"
        "s_add_u32 s60, s60, s63\n"
        "s_addc_u32 s61, s61, 0\n"
        "global_load_lds_dwordx4 v8, s[60:61]\n"
        "s_add_u32 m0, m0, s62\n"
        "s_add_u32 s60, s60, s63\n"
        "s_addc_u32 s61, s61, 0\n"
        "global_load_lds_dwordx4 v8, s[60:61]\n"
        "s_add_u32 m0, m0, s62\n"
        "s_add_u32 s60, s60, s63\n"
        "s_addc_u32 s61, s61, 0\n"
        "global_load_lds_dwordx4 v8, s[60:61]\n"
        "s_add_u32 m0, m0, s62\n"
        "s_add_u32 s60, s60, s63\n"
        "s_addc_u32 s61, s61, 0\n"
        "global_load_lds_dwordx4 v8, s[60:61]\n"
        "s_add_u32 m0, m0, s62\n"
        "s_add_u32 s60, s60, s63\n"
        "s_addc_u32 s61, s61, 0\n"
        "global_load_lds_dwordx4 v8, s[60:61]\n"
        "s_add_u32 m0, m0, s62\n"
        "s_add_u32 s60, s60, s63\n"
        "s_addc_u32 s61, s61, 0\n"
        "global_load_lds_dwordx4 v8, s[60:61]\n"
        "s_add_u32 m0, m0, s62\n"
        "s_add_u32 s60, s60, s63\n"
        "s_addc_u32 s61, s61, 0\n"
        "global_load_lds_dwordx4 v8, s[60:61]\n"
        "s_add_u32 m0, m0, s62\n"
        "s_add_u32 s60, s60, s63\n"
        "s_addc_u32 s61, s61, 0\n"
        "global_load_lds_dwordx4 v8, s[60:61]\n"
        "s_add_u32 m0, m0, s62\n"
        "s_add_u32 s60, s60, s63\n"
        "s_addc_u32 s61, s61, 0\n"
        "global_load_lds_dwordx4 v8, s[60:61]\n"
        "s_add_u32 m0, m0, s62\n"
        "s_add_u32 s60, s60, s63\n"
        "s_addc_u32 s61, s61, 0\n"
        "global_load_lds_dwordx4 v8, s[60:61]\n"
        "s_add_u32 m0, m0, s62\n"
        "s_add_u32 s60, s60, s63\n"
        "s_addc_u32 s61, s61, 0\n"
        "global_load_lds_dwordx4 v8, s[60:61]\n"
        "s_add_u32 m0, m0, s62\n"
        "s_add_u32 s60, s60, s63\n"
        "s_addc_u32 s61, s61, 0\n"
        "v_bfe_u32 v16, v17, 2, 2\n"
        "v_lshl_add_u32 v14, v16, 1, 0\n"
        "v_bfe_u32 v16, v17, 4, 1\n"
        "v_lshl_add_u32 v14, v16, 3, v14\n"
        "s_lshl_b32 s64, s35, 2\n"
        "v_bfe_u32 v16, v17, 1, 1\n"
        "v_lshl_add_u32 v15, v16, 0, s64\n"
        "v_xor_b32 v14, v14, v15\n"
        "v_lshlrev_b32 v14, 4, v14\n"
        "v_bfe_u32 v16, v17, 4, 2\n"
        "v_lshl_add_u32 v0, v16, 11, v14\n"
        "v_bfe_u32 v16, v17, 2, 2\n"
        "v_lshl_add_u32 v0, v16, 8, v0\n"
        "v_bfe_u32 v16, v17, 0, 1\n"
        "v_lshl_add_u32 v0, v16, 3, v0\n"
        "v_xor_b32 v1, 32, v0\n"
        "v_add_u32 v2, 0x10000, v0\n"
        "v_add_u32 v3, 0x10000, v1\n"
        "v_lshlrev_b32 v9, 4, v17\n"
        "v_add_u32 v11, 0x1000, v9\n"
        "v_lshlrev_b32 v19, 7, v17\n"
        "v_add_u32 v19, 24576, v19\n"
        "v_bfe_u32 v16, v17, 4, 2\n"
        "v_lshl_add_u32 v21, v16, 8, 0\n"
        "v_bfe_u32 v16, v17, 1, 1\n"
        "v_lshl_add_u32 v21, v16, 7, v21\n"
        "v_bfe_u32 v16, v17, 2, 2\n"
        "v_lshl_add_u32 v21, v16, 5, v21\n"
        "v_bfe_u32 v16, v17, 0, 1\n"
        "v_lshl_add_u32 v21, v16, 4, v21\n"
        "s_cmp_eq_u32 s36, 1\n"
        "s_cselect_b64 vcc, -1, 0\n"
        "s_mov_b32 s65, 37748736\n"
        "s_cselect_b32 s48, 4194304, s65\n"
        "v_cndmask_b32 v20, v9, v21, vcc\n"
        "s_cmp_eq_u32 s36, 2\n"
        "s_cselect_b32 s48, 20971520, s48\n"
        "s_cselect_b32 s49, 11, 10\n"
        "s_cselect_b32 s40, 0x400, s62\n"
        "s_lshl_b32 s49, s35, s49\n"
        "s_add_u32 s48, s48, s49\n"
        "v_add_u32 v21, s40, v20\n"
        "s_mul_i32 s64, s34, 8192\n"
        "s_add_u32 s42, s22, s64\n"
        "s_addc_u32 s43, s23, 0\n"
        "s_add_u32 s42, s42, s48\n"
        "s_addc_u32 s43, s43, 0\n"
        "s_mul_i32 s64, s34, 128\n"
        "s_add_u32 s64, s64, 0x1000\n"
        "s_add_u32 s44, s20, s64\n"
        "s_addc_u32 s45, s21, 0\n"
        "s_lshl_b32 s41, s36, 12\n"
        "s_add_u32 s44, s44, s41\n"
        "s_addc_u32 s45, s45, 0\n"
        "s_cmp_eq_u32 s35, 0\n"
        "s_cselect_b64 s[56:57], 1, 0\n"
        "s_mov_b32 s64, 0\n"
        "s_mul_i32 s65, s35, 8192\n"
        "s_add_u32 s64, s64, s65\n"
        "s_add_u32 s64, s64, 0x200000\n"
        "s_add_u32 s46, s22, s64\n"
        "s_addc_u32 s47, s23, 0\n"
        "s_lshl_b32 s39, s35, 13\n"
        "s_mov_b32 s50, 0\n"
        "s_waitcnt vmcnt(16)\n"
        "s_barrier\n"
        "global_load_dword v25, v18, s[20:21] sc1\n"
        "global_load_lds_dwordx4 v8, s[60:61]\n"
        "s_add_u32 m0, m0, s62\n"
        "s_add_u32 s60, s60, s63\n"
        "s_addc_u32 s61, s61, 0\n"
        "global_load_lds_dwordx4 v8, s[60:61]\n"
        "s_add_u32 m0, m0, s62\n"
        "s_add_u32 s60, s60, s63\n"
        "s_addc_u32 s61, s61, 0\n"
        "global_load_lds_dwordx4 v8, s[60:61]\n"
        "s_add_u32 m0, m0, s62\n"
        "s_add_u32 s60, s60, s63\n"
        "s_addc_u32 s61, s61, 0\n"
        "global_load_lds_dwordx4 v8, s[60:61]\n"
        "s_add_u32 m0, m0, s62\n"
        "s_add_u32 s60, s60, s63\n"
        "s_addc_u32 s61, s61, 0\n"
        "global_load_lds_dwordx4 v8, s[60:61]\n"
        "s_add_u32 m0, m0, s62\n"
        "s_add_u32 s60, s60, s63\n"
        "s_addc_u32 s61, s61, 0\n"
        "global_load_lds_dwordx4 v8, s[60:61]\n"
        "s_add_u32 m0, m0, s62\n"
        "s_add_u32 s60, s60, s63\n"
        "s_addc_u32 s61, s61, 0\n"
        "global_load_lds_dwordx4 v8, s[60:61]\n"
        "s_add_u32 m0, m0, s62\n"
        "s_add_u32 s60, s60, s63\n"
        "s_addc_u32 s61, s61, 0\n"
        "global_load_lds_dwordx4 v8, s[60:61]\n"
        "s_add_u32 m0, m0, s62\n"
        "s_add_u32 s60, s60, s63\n"
        "s_addc_u32 s61, s61, 0\n"
        "ds_read_b64_tr_b16 a[0:1], v0 offset:0\n"
        "ds_read_b64_tr_b16 a[2:3], v0 offset:1024\n"
        "ds_read_b64_tr_b16 a[64:65], v1 offset:0\n"
        "ds_read_b64_tr_b16 a[66:67], v1 offset:1024\n"
        "ds_read_b64_tr_b16 a[4:5], v0 offset:8192\n"
        "ds_read_b64_tr_b16 a[6:7], v0 offset:9216\n"
        "ds_read_b64_tr_b16 a[68:69], v1 offset:8192\n"
        "ds_read_b64_tr_b16 a[70:71], v1 offset:9216\n"
        "ds_read_b64_tr_b16 a[8:9], v0 offset:16384\n"
        "ds_read_b64_tr_b16 a[10:11], v0 offset:17408\n"
        "ds_read_b64_tr_b16 a[72:73], v1 offset:16384\n"
        "ds_read_b64_tr_b16 a[74:75], v1 offset:17408\n"
        "ds_read_b64_tr_b16 a[12:13], v0 offset:24576\n"
        "ds_read_b64_tr_b16 a[14:15], v0 offset:25600\n"
        "ds_read_b64_tr_b16 a[76:77], v1 offset:24576\n"
        "ds_read_b64_tr_b16 a[78:79], v1 offset:25600\n"
        "s_waitcnt vmcnt(17)\n"
        "s_barrier\n"
        "ds_read_b64_tr_b16 a[16:17], v0 offset:32768\n"
        "ds_read_b64_tr_b16 a[18:19], v0 offset:33792\n"
        "ds_read_b64_tr_b16 a[80:81], v1 offset:32768\n"
        "ds_read_b64_tr_b16 a[82:83], v1 offset:33792\n"
        "ds_read_b64_tr_b16 a[20:21], v0 offset:40960\n"
        "ds_read_b64_tr_b16 a[22:23], v0 offset:41984\n"
        "ds_read_b64_tr_b16 a[84:85], v1 offset:40960\n"
        "ds_read_b64_tr_b16 a[86:87], v1 offset:41984\n"
        "ds_read_b64_tr_b16 a[24:25], v0 offset:49152\n"
        "ds_read_b64_tr_b16 a[26:27], v0 offset:50176\n"
        "ds_read_b64_tr_b16 a[88:89], v1 offset:49152\n"
        "ds_read_b64_tr_b16 a[90:91], v1 offset:50176\n"
        "ds_read_b64_tr_b16 a[28:29], v0 offset:57344\n"
        "ds_read_b64_tr_b16 a[30:31], v0 offset:58368\n"
        "ds_read_b64_tr_b16 a[92:93], v1 offset:57344\n"
        "ds_read_b64_tr_b16 a[94:95], v1 offset:58368\n"
        "s_waitcnt vmcnt(8)\n"
        "s_barrier\n"
        "v_readfirstlane_b32 s52, v24\n"
        "s_cmp_eq_u32 s37, 0\n"
        "s_cbranch_scc1 Lt1%=\n"
        "Lt0%=:\n"
        "v_readfirstlane_b32 s65, v25\n"
        "s_cmp_eq_u32 s65, s52\n"
        "s_cbranch_scc0 Lt1%=\n"
        "s_add_u32 s67, s67, 1\n"
        "s_cmp_gt_u32 s67, 262144\n"
        "s_cbranch_scc1 Lfail%=\n"
        "global_load_dword v25, v18, s[20:21] sc1\n"
        "s_waitcnt vmcnt(0)\n"
        "s_branch Lt0%=\n"
        "Lt1%=:\n"
        "global_load_dwordx2 v[22:23], v19, s[20:21] sc1\n"
        "ds_read_b64_tr_b16 a[32:33], v2 offset:0\n"
        "ds_read_b64_tr_b16 a[34:35], v2 offset:1024\n"
        "ds_read_b64_tr_b16 a[96:97], v3 offset:0\n"
        "ds_read_b64_tr_b16 a[98:99], v3 offset:1024\n"
        "ds_read_b64_tr_b16 a[36:37], v2 offset:8192\n"
        "ds_read_b64_tr_b16 a[38:39], v2 offset:9216\n"
        "ds_read_b64_tr_b16 a[100:101], v3 offset:8192\n"
        "ds_read_b64_tr_b16 a[102:103], v3 offset:9216\n"
        "ds_read_b64_tr_b16 a[40:41], v2 offset:16384\n"
        "ds_read_b64_tr_b16 a[42:43], v2 offset:17408\n"
        "ds_read_b64_tr_b16 a[104:105], v3 offset:16384\n"
        "ds_read_b64_tr_b16 a[106:107], v3 offset:17408\n"
        "ds_read_b64_tr_b16 a[44:45], v2 offset:24576\n"
        "ds_read_b64_tr_b16 a[46:47], v2 offset:25600\n"
        "ds_read_b64_tr_b16 a[108:109], v3 offset:24576\n"
        "ds_read_b64_tr_b16 a[110:111], v3 offset:25600\n"
        "s_waitcnt vmcnt(0) lgkmcnt(0)\n"
        "s_barrier\n"
        "v_and_b32 v22, v22, v23\n"
        "v_cmp_ne_u32 s[52:53], 0, v22\n"
        "s_mov_b32 s54, 0x8000\n"
        "s_mov_b32 s51, 0\n"
        "Ln0a%=:\n"
        "s_bitcmp1_b64 s[52:53], 0\n"
        "s_cbranch_scc1 Ln0b%=\n"
        "s_add_u32 s67, s67, 1\n"
        "s_cmp_gt_u32 s67, 262144\n"
        "s_cbranch_scc1 Lfail%=\n"
        "s_sleep 1\n"
        "global_load_dwordx2 v[22:23], v19, s[20:21] sc1\n"
        "s_waitcnt vmcnt(0)\n"
        "v_and_b32 v22, v22, v23\n"
        "v_cmp_ne_u32 s[52:53], 0, v22\n"
        "s_branch Ln0a%=\n"
        "Ln0b%=:\n"
        "s_add_u32 m0, s51, s39\n"
        "s_nop 0\n"
        "global_load_lds_dwordx4 v9, s[46:47] offset:0 sc1\n"
        "global_load_lds_dwordx4 v9, s[46:47] offset:1024 sc1\n"
        "global_load_lds_dwordx4 v9, s[46:47] offset:2048 sc1\n"
        "global_load_lds_dwordx4 v9, s[46:47] offset:3072 sc1\n"
        "s_add_u32 m0, m0, s62\n"
        "s_nop 0\n"
        "global_load_lds_dwordx4 v11, s[46:47] offset:0 sc1\n"
        "global_load_lds_dwordx4 v11, s[46:47] offset:1024 sc1\n"
        "global_load_lds_dwordx4 v11, s[46:47] offset:2048 sc1\n"
        "global_load_lds_dwordx4 v11, s[46:47] offset:3072 sc1\n"
        "s_add_u32 s46, s46, s54\n"
        "s_addc_u32 s47, s47, 0\n"
        "ds_read_b64_tr_b16 a[48:49], v2 offset:32768\n"
        "ds_read_b64_tr_b16 a[50:51], v2 offset:33792\n"
        "ds_read_b64_tr_b16 a[112:113], v3 offset:32768\n"
        "ds_read_b64_tr_b16 a[114:115], v3 offset:33792\n"
        "ds_read_b64_tr_b16 a[52:53], v2 offset:40960\n"
        "ds_read_b64_tr_b16 a[54:55], v2 offset:41984\n"
        "ds_read_b64_tr_b16 a[116:117], v3 offset:40960\n"
        "ds_read_b64_tr_b16 a[118:119], v3 offset:41984\n"
        "ds_read_b64_tr_b16 a[56:57], v2 offset:49152\n"
        "ds_read_b64_tr_b16 a[58:59], v2 offset:50176\n"
        "ds_read_b64_tr_b16 a[120:121], v3 offset:49152\n"
        "ds_read_b64_tr_b16 a[122:123], v3 offset:50176\n"
        "ds_read_b64_tr_b16 a[60:61], v2 offset:57344\n"
        "ds_read_b64_tr_b16 a[62:63], v2 offset:58368\n"
        "ds_read_b64_tr_b16 a[124:125], v3 offset:57344\n"
        "ds_read_b64_tr_b16 a[126:127], v3 offset:58368\n"
        "s_mov_b32 s51, 0x8000\n"
        "Ln1a%=:\n"
        "s_bitcmp1_b64 s[52:53], 1\n"
        "s_cbranch_scc1 Ln1b%=\n"
        "s_add_u32 s67, s67, 1\n"
        "s_cmp_gt_u32 s67, 262144\n"
        "s_cbranch_scc1 Lfail%=\n"
        "s_sleep 1\n"
        "global_load_dwordx2 v[22:23], v19, s[20:21] sc1\n"
        "s_waitcnt vmcnt(0)\n"
        "v_and_b32 v22, v22, v23\n"
        "v_cmp_ne_u32 s[52:53], 0, v22\n"
        "s_branch Ln1a%=\n"
        "Ln1b%=:\n"
        "s_add_u32 m0, s51, s39\n"
        "s_nop 0\n"
        "global_load_lds_dwordx4 v9, s[46:47] offset:0 sc1\n"
        "global_load_lds_dwordx4 v9, s[46:47] offset:1024 sc1\n"
        "global_load_lds_dwordx4 v9, s[46:47] offset:2048 sc1\n"
        "global_load_lds_dwordx4 v9, s[46:47] offset:3072 sc1\n"
        "s_add_u32 m0, m0, s62\n"
        "s_nop 0\n"
        "global_load_lds_dwordx4 v11, s[46:47] offset:0 sc1\n"
        "global_load_lds_dwordx4 v11, s[46:47] offset:1024 sc1\n"
        "global_load_lds_dwordx4 v11, s[46:47] offset:2048 sc1\n"
        "global_load_lds_dwordx4 v11, s[46:47] offset:3072 sc1\n"
        "s_add_u32 s46, s46, s54\n"
        "s_addc_u32 s47, s47, 0\n"
        "s_cmp_eq_u32 s35, 0\n"
        "s_cselect_b32 s64, s37, 0\n"
        "s_cmp_eq_u32 s64, 0\n"
        "s_cbranch_scc1 Lna%=\n"
        "v_mov_b32 v22, 1\n"
        "v_mov_b32 v23, s38\n"
        "s_mov_b64 exec, 1\n"
        "global_atomic_add v23, v22, s[20:21]\n"
        "s_mov_b64 exec, -1\n"
        "s_waitcnt vmcnt(9)\n"
        "s_branch Lnb%=\n"
        "Lna%=:\n"
        "s_waitcnt vmcnt(8)\n"
        "Lnb%=:\n"
        "s_cmp_eq_u32 s36, 2\n"
        "s_cbranch_scc1 Lv%=\n"
        "Lk%=:\n"
        "s_and_b32 s64, s50, 3\n"
        "s_lshl_b32 s64, s64, 15\n"
        "v_add_u32 v10, s64, v9\n"
        "s_xor_b32 s51, s64, 0x10000\n"
        "s_waitcnt lgkmcnt(0)\n"
        "s_barrier\n"
        "ds_read_b128 v[128:131], v10 offset:0\n"
        "ds_read_b128 v[132:135], v10 offset:16384\n"
        "ds_read_b128 v[136:139], v10 offset:1024\n"
        "ds_read_b128 v[140:143], v10 offset:17408\n"
        "ds_read_b128 v[144:147], v10 offset:2048\n"
        "ds_read_b128 v[148:151], v10 offset:18432\n"
        "s_add_u32 s55, s50, 2\n"
        "s_min_u32 s55, s55, 63\n"
        "s_cmp_lt_u32 s50, 61\n"
        "s_cselect_b32 s54, s54, 0\n"
        "v_mov_b32 v24, s50\n"
        "Lnka%=:\n"
        "s_bitcmp1_b64 s[52:53], s55\n"
        "s_cbranch_scc1 Lnkb%=\n"
        "s_add_u32 s67, s67, 1\n"
        "s_cmp_gt_u32 s67, 262144\n"
        "s_cbranch_scc1 Lfail%=\n"
        "s_sleep 1\n"
        "global_load_dwordx2 v[22:23], v19, s[20:21] sc1\n"
        "s_waitcnt vmcnt(0)\n"
        "v_and_b32 v22, v22, v23\n"
        "v_cmp_ne_u32 s[52:53], 0, v22\n"
        "s_branch Lnka%=\n"
        "Lnkb%=:\n"
        "s_waitcnt lgkmcnt(5)\n"
        "v_mfma_f32_16x16x32_bf16 v[64:67], a[0:3], v[128:131], 0\n"
        "ds_read_b128 v[152:155], v10 offset:3072\n"
        "v_mfma_f32_16x16x32_bf16 v[68:71], a[64:67], v[128:131], 0\n"
        "s_add_u32 m0, s51, s39\n"
        "s_waitcnt lgkmcnt(5)\n"
        "v_mfma_f32_16x16x32_bf16 v[72:75], a[0:3], v[132:135], 0\n"
        "ds_read_b128 v[156:159], v10 offset:19456\n"
        "v_mfma_f32_16x16x32_bf16 v[76:79], a[64:67], v[132:135], 0\n"
        "s_waitcnt lgkmcnt(5)\n"
        "v_mfma_f32_16x16x32_bf16 v[64:67], a[4:7], v[136:139], v[64:67]\n"
        "ds_read_b128 v[128:131], v10 offset:4096\n"
        "v_mfma_f32_16x16x32_bf16 v[68:71], a[68:71], v[136:139], v[68:71]\n"
        "global_load_lds_dwordx4 v9, s[46:47] offset:0 sc1\n"
        "s_waitcnt lgkmcnt(5)\n"
        "v_mfma_f32_16x16x32_bf16 v[72:75], a[4:7], v[140:143], v[72:75]\n"
        "ds_read_b128 v[132:135], v10 offset:20480\n"
        "v_mfma_f32_16x16x32_bf16 v[76:79], a[68:71], v[140:143], v[76:79]\n"
        "s_waitcnt lgkmcnt(5)\n"
        "v_mfma_f32_16x16x32_bf16 v[64:67], a[8:11], v[144:147], v[64:67]\n"
        "ds_read_b128 v[136:139], v10 offset:5120\n"
        "v_mfma_f32_16x16x32_bf16 v[68:71], a[72:75], v[144:147], v[68:71]\n"
        "global_load_lds_dwordx4 v9, s[46:47] offset:1024 sc1\n"
        "s_waitcnt lgkmcnt(5)\n"
        "v_mfma_f32_16x16x32_bf16 v[72:75], a[8:11], v[148:151], v[72:75]\n"
        "ds_read_b128 v[140:143], v10 offset:21504\n"
        "v_mfma_f32_16x16x32_bf16 v[76:79], a[72:75], v[148:151], v[76:79]\n"
        "s_waitcnt lgkmcnt(5)\n"
        "v_mfma_f32_16x16x32_bf16 v[64:67], a[12:15], v[152:155], v[64:67]\n"
        "ds_read_b128 v[144:147], v10 offset:6144\n"
        "v_mfma_f32_16x16x32_bf16 v[68:71], a[76:79], v[152:155], v[68:71]\n"
        "global_load_lds_dwordx4 v9, s[46:47] offset:2048 sc1\n"
        "s_waitcnt lgkmcnt(5)\n"
        "v_mfma_f32_16x16x32_bf16 v[72:75], a[12:15], v[156:159], v[72:75]\n"
        "ds_read_b128 v[148:151], v10 offset:22528\n"
        "v_mfma_f32_16x16x32_bf16 v[76:79], a[76:79], v[156:159], v[76:79]\n"
        "s_waitcnt lgkmcnt(5)\n"
        "v_mfma_f32_16x16x32_bf16 v[64:67], a[16:19], v[128:131], v[64:67]\n"
        "ds_read_b128 v[152:155], v10 offset:7168\n"
        "v_mfma_f32_16x16x32_bf16 v[68:71], a[80:83], v[128:131], v[68:71]\n"
        "global_load_lds_dwordx4 v9, s[46:47] offset:3072 sc1\n"
        "s_add_u32 m0, m0, s62\n"
        "s_waitcnt lgkmcnt(5)\n"
        "v_mfma_f32_16x16x32_bf16 v[72:75], a[16:19], v[132:135], v[72:75]\n"
        "ds_read_b128 v[156:159], v10 offset:23552\n"
        "v_mfma_f32_16x16x32_bf16 v[76:79], a[80:83], v[132:135], v[76:79]\n"
        "s_waitcnt lgkmcnt(5)\n"
        "v_mfma_f32_16x16x32_bf16 v[64:67], a[20:23], v[136:139], v[64:67]\n"
        "ds_read_b128 v[128:131], v10 offset:8192\n"
        "v_mfma_f32_16x16x32_bf16 v[68:71], a[84:87], v[136:139], v[68:71]\n"
        "global_load_lds_dwordx4 v11, s[46:47] offset:0 sc1\n"
        "s_waitcnt lgkmcnt(5)\n"
        "v_mfma_f32_16x16x32_bf16 v[72:75], a[20:23], v[140:143], v[72:75]\n"
        "ds_read_b128 v[132:135], v10 offset:24576\n"
        "v_mfma_f32_16x16x32_bf16 v[76:79], a[84:87], v[140:143], v[76:79]\n"
        "s_waitcnt lgkmcnt(5)\n"
        "v_mfma_f32_16x16x32_bf16 v[64:67], a[24:27], v[144:147], v[64:67]\n"
        "ds_read_b128 v[136:139], v10 offset:9216\n"
        "v_mfma_f32_16x16x32_bf16 v[68:71], a[88:91], v[144:147], v[68:71]\n"
        "global_load_lds_dwordx4 v11, s[46:47] offset:1024 sc1\n"
        "s_waitcnt lgkmcnt(5)\n"
        "v_mfma_f32_16x16x32_bf16 v[72:75], a[24:27], v[148:151], v[72:75]\n"
        "ds_read_b128 v[140:143], v10 offset:25600\n"
        "v_mfma_f32_16x16x32_bf16 v[76:79], a[88:91], v[148:151], v[76:79]\n"
        "s_waitcnt lgkmcnt(5)\n"
        "v_mfma_f32_16x16x32_bf16 v[64:67], a[28:31], v[152:155], v[64:67]\n"
        "ds_read_b128 v[144:147], v10 offset:10240\n"
        "v_mfma_f32_16x16x32_bf16 v[68:71], a[92:95], v[152:155], v[68:71]\n"
        "global_load_lds_dwordx4 v11, s[46:47] offset:2048 sc1\n"
        "s_waitcnt lgkmcnt(5)\n"
        "v_mfma_f32_16x16x32_bf16 v[72:75], a[28:31], v[156:159], v[72:75]\n"
        "ds_read_b128 v[148:151], v10 offset:26624\n"
        "v_mfma_f32_16x16x32_bf16 v[76:79], a[92:95], v[156:159], v[76:79]\n"
        "s_waitcnt lgkmcnt(5)\n"
        "v_mfma_f32_16x16x32_bf16 v[64:67], a[32:35], v[128:131], v[64:67]\n"
        "ds_read_b128 v[152:155], v10 offset:11264\n"
        "v_mfma_f32_16x16x32_bf16 v[68:71], a[96:99], v[128:131], v[68:71]\n"
        "global_load_lds_dwordx4 v11, s[46:47] offset:3072 sc1\n"
        "s_add_u32 s46, s46, s54\n"
        "s_addc_u32 s47, s47, 0\n"
        "s_waitcnt lgkmcnt(5)\n"
        "v_mfma_f32_16x16x32_bf16 v[72:75], a[32:35], v[132:135], v[72:75]\n"
        "ds_read_b128 v[156:159], v10 offset:27648\n"
        "v_mfma_f32_16x16x32_bf16 v[76:79], a[96:99], v[132:135], v[76:79]\n"
        "s_waitcnt lgkmcnt(5)\n"
        "v_mfma_f32_16x16x32_bf16 v[64:67], a[36:39], v[136:139], v[64:67]\n"
        "ds_read_b128 v[128:131], v10 offset:12288\n"
        "v_mfma_f32_16x16x32_bf16 v[68:71], a[100:103], v[136:139], v[68:71]\n"
        "s_waitcnt lgkmcnt(5)\n"
        "v_mfma_f32_16x16x32_bf16 v[72:75], a[36:39], v[140:143], v[72:75]\n"
        "ds_read_b128 v[132:135], v10 offset:28672\n"
        "v_mfma_f32_16x16x32_bf16 v[76:79], a[100:103], v[140:143], v[76:79]\n"
        "s_waitcnt lgkmcnt(5)\n"
        "v_mfma_f32_16x16x32_bf16 v[64:67], a[40:43], v[144:147], v[64:67]\n"
        "ds_read_b128 v[136:139], v10 offset:13312\n"
        "v_mfma_f32_16x16x32_bf16 v[68:71], a[104:107], v[144:147], v[68:71]\n"
        "s_waitcnt lgkmcnt(5)\n"
        "v_mfma_f32_16x16x32_bf16 v[72:75], a[40:43], v[148:151], v[72:75]\n"
        "ds_read_b128 v[140:143], v10 offset:29696\n"
        "v_mfma_f32_16x16x32_bf16 v[76:79], a[104:107], v[148:151], v[76:79]\n"
        "s_waitcnt lgkmcnt(5)\n"
        "v_mfma_f32_16x16x32_bf16 v[64:67], a[44:47], v[152:155], v[64:67]\n"
        "ds_read_b128 v[144:147], v10 offset:14336\n"
        "v_mfma_f32_16x16x32_bf16 v[68:71], a[108:111], v[152:155], v[68:71]\n"
        "s_waitcnt vmcnt(8)\n"
        "s_barrier\n"
        "s_mov_b64 exec, s[56:57]\n"
        "s_cbranch_execz Lfk%=\n"
        "global_store_dword v18, v24, s[44:45] sc1\n"
        "Lfk%=:\n"
        "s_mov_b64 exec, -1\n"
        "s_waitcnt lgkmcnt(5)\n"
        "v_mfma_f32_16x16x32_bf16 v[72:75], a[44:47], v[156:159], v[72:75]\n"
        "ds_read_b128 v[148:151], v10 offset:30720\n"
        "v_mfma_f32_16x16x32_bf16 v[76:79], a[108:111], v[156:159], v[76:79]\n"
        "s_waitcnt lgkmcnt(5)\n"
        "v_mfma_f32_16x16x32_bf16 v[64:67], a[48:51], v[128:131], v[64:67]\n"
        "ds_read_b128 v[152:155], v10 offset:15360\n"
        "v_mfma_f32_16x16x32_bf16 v[68:71], a[112:115], v[128:131], v[68:71]\n"
        "s_waitcnt lgkmcnt(5)\n"
        "v_mfma_f32_16x16x32_bf16 v[72:75], a[48:51], v[132:135], v[72:75]\n"
        "ds_read_b128 v[156:159], v10 offset:31744\n"
        "v_mfma_f32_16x16x32_bf16 v[76:79], a[112:115], v[132:135], v[76:79]\n"
        "s_waitcnt lgkmcnt(5)\n"
        "v_mfma_f32_16x16x32_bf16 v[64:67], a[52:55], v[136:139], v[64:67]\n"
        "v_mfma_f32_16x16x32_bf16 v[68:71], a[116:119], v[136:139], v[68:71]\n"
        "s_waitcnt lgkmcnt(4)\n"
        "v_mfma_f32_16x16x32_bf16 v[72:75], a[52:55], v[140:143], v[72:75]\n"
        "v_mfma_f32_16x16x32_bf16 v[76:79], a[116:119], v[140:143], v[76:79]\n"
        "s_waitcnt lgkmcnt(3)\n"
        "v_mfma_f32_16x16x32_bf16 v[64:67], a[56:59], v[144:147], v[64:67]\n"
        "v_mfma_f32_16x16x32_bf16 v[68:71], a[120:123], v[144:147], v[68:71]\n"
        "s_waitcnt lgkmcnt(2)\n"
        "v_mfma_f32_16x16x32_bf16 v[72:75], a[56:59], v[148:151], v[72:75]\n"
        "v_mfma_f32_16x16x32_bf16 v[76:79], a[120:123], v[148:151], v[76:79]\n"
        "s_waitcnt lgkmcnt(1)\n"
        "v_mfma_f32_16x16x32_bf16 v[64:67], a[60:63], v[152:155], v[64:67]\n"
        "v_mfma_f32_16x16x32_bf16 v[68:71], a[124:127], v[152:155], v[68:71]\n"
        "s_waitcnt lgkmcnt(0)\n"
        "v_mfma_f32_16x16x32_bf16 v[72:75], a[60:63], v[156:159], v[72:75]\n"
        "v_mfma_f32_16x16x32_bf16 v[76:79], a[124:127], v[156:159], v[76:79]\n"
        "s_add_u32 s50, s50, 1\n"
        "s_nop 4\n"
        "v_cvt_pk_bf16_f32 v96, v64, v68\n"
        "v_cvt_pk_bf16_f32 v97, v65, v69\n"
        "v_cvt_pk_bf16_f32 v98, v66, v70\n"
        "v_cvt_pk_bf16_f32 v99, v67, v71\n"
        "global_store_dwordx4 v20, v[96:99], s[42:43] sc1\n"
        "v_cvt_pk_bf16_f32 v100, v72, v76\n"
        "v_cvt_pk_bf16_f32 v101, v73, v77\n"
        "v_cvt_pk_bf16_f32 v102, v74, v78\n"
        "v_cvt_pk_bf16_f32 v103, v75, v79\n"
        "global_store_dwordx4 v21, v[100:103], s[42:43] sc1\n"
        "s_add_u32 s42, s42, 0x40000\n"
        "s_addc_u32 s43, s43, 0\n"
        "s_cmp_lt_u32 s50, 8\n"
        "s_cbranch_scc1 Lk%=\n"
        "s_sleep 0\n"
        "s_cmp_lt_u32 s50, 64\n"
        "s_cbranch_scc1 Lk%=\n"
        "s_branch Lfail%=\n"
        "Lv%=:\n"
        "s_and_b32 s64, s50, 3\n"
        "s_lshl_b32 s64, s64, 15\n"
        "v_add_u32 v10, s64, v9\n"
        "s_xor_b32 s51, s64, 0x10000\n"
        "s_waitcnt lgkmcnt(0)\n"
        "s_barrier\n"
        "ds_read_b128 v[128:131], v10 offset:0\n"
        "ds_read_b128 v[132:135], v10 offset:16384\n"
        "ds_read_b128 v[136:139], v10 offset:1024\n"
        "ds_read_b128 v[140:143], v10 offset:17408\n"
        "ds_read_b128 v[144:147], v10 offset:2048\n"
        "ds_read_b128 v[148:151], v10 offset:18432\n"
        "s_add_u32 s55, s50, 2\n"
        "s_min_u32 s55, s55, 63\n"
        "s_cmp_lt_u32 s50, 61\n"
        "s_cselect_b32 s54, s54, 0\n"
        "v_mov_b32 v24, s50\n"
        "Lnva%=:\n"
        "s_bitcmp1_b64 s[52:53], s55\n"
        "s_cbranch_scc1 Lnvb%=\n"
        "s_add_u32 s67, s67, 1\n"
        "s_cmp_gt_u32 s67, 262144\n"
        "s_cbranch_scc1 Lfail%=\n"
        "s_sleep 1\n"
        "global_load_dwordx2 v[22:23], v19, s[20:21] sc1\n"
        "s_waitcnt vmcnt(0)\n"
        "v_and_b32 v22, v22, v23\n"
        "v_cmp_ne_u32 s[52:53], 0, v22\n"
        "s_branch Lnva%=\n"
        "Lnvb%=:\n"
        "s_waitcnt lgkmcnt(5)\n"
        "v_mfma_f32_16x16x32_bf16 v[64:67], v[128:131], a[0:3], 0\n"
        "ds_read_b128 v[152:155], v10 offset:3072\n"
        "v_mfma_f32_16x16x32_bf16 v[68:71], v[128:131], a[64:67], 0\n"
        "s_add_u32 m0, s51, s39\n"
        "s_waitcnt lgkmcnt(5)\n"
        "v_mfma_f32_16x16x32_bf16 v[72:75], v[132:135], a[0:3], 0\n"
        "ds_read_b128 v[156:159], v10 offset:19456\n"
        "v_mfma_f32_16x16x32_bf16 v[76:79], v[132:135], a[64:67], 0\n"
        "s_waitcnt lgkmcnt(5)\n"
        "v_mfma_f32_16x16x32_bf16 v[64:67], v[136:139], a[4:7], v[64:67]\n"
        "ds_read_b128 v[128:131], v10 offset:4096\n"
        "v_mfma_f32_16x16x32_bf16 v[68:71], v[136:139], a[68:71], v[68:71]\n"
        "global_load_lds_dwordx4 v9, s[46:47] offset:0 sc1\n"
        "s_waitcnt lgkmcnt(5)\n"
        "v_mfma_f32_16x16x32_bf16 v[72:75], v[140:143], a[4:7], v[72:75]\n"
        "ds_read_b128 v[132:135], v10 offset:20480\n"
        "v_mfma_f32_16x16x32_bf16 v[76:79], v[140:143], a[68:71], v[76:79]\n"
        "s_waitcnt lgkmcnt(5)\n"
        "v_mfma_f32_16x16x32_bf16 v[64:67], v[144:147], a[8:11], v[64:67]\n"
        "ds_read_b128 v[136:139], v10 offset:5120\n"
        "v_mfma_f32_16x16x32_bf16 v[68:71], v[144:147], a[72:75], v[68:71]\n"
        "global_load_lds_dwordx4 v9, s[46:47] offset:1024 sc1\n"
        "s_waitcnt lgkmcnt(5)\n"
        "v_mfma_f32_16x16x32_bf16 v[72:75], v[148:151], a[8:11], v[72:75]\n"
        "ds_read_b128 v[140:143], v10 offset:21504\n"
        "v_mfma_f32_16x16x32_bf16 v[76:79], v[148:151], a[72:75], v[76:79]\n"
        "s_waitcnt lgkmcnt(5)\n"
        "v_mfma_f32_16x16x32_bf16 v[64:67], v[152:155], a[12:15], v[64:67]\n"
        "ds_read_b128 v[144:147], v10 offset:6144\n"
        "v_mfma_f32_16x16x32_bf16 v[68:71], v[152:155], a[76:79], v[68:71]\n"
        "global_load_lds_dwordx4 v9, s[46:47] offset:2048 sc1\n"
        "s_waitcnt lgkmcnt(5)\n"
        "v_mfma_f32_16x16x32_bf16 v[72:75], v[156:159], a[12:15], v[72:75]\n"
        "ds_read_b128 v[148:151], v10 offset:22528\n"
        "v_mfma_f32_16x16x32_bf16 v[76:79], v[156:159], a[76:79], v[76:79]\n"
        "s_waitcnt lgkmcnt(5)\n"
        "v_mfma_f32_16x16x32_bf16 v[64:67], v[128:131], a[16:19], v[64:67]\n"
        "ds_read_b128 v[152:155], v10 offset:7168\n"
        "v_mfma_f32_16x16x32_bf16 v[68:71], v[128:131], a[80:83], v[68:71]\n"
        "global_load_lds_dwordx4 v9, s[46:47] offset:3072 sc1\n"
        "s_add_u32 m0, m0, s62\n"
        "s_waitcnt lgkmcnt(5)\n"
        "v_mfma_f32_16x16x32_bf16 v[72:75], v[132:135], a[16:19], v[72:75]\n"
        "ds_read_b128 v[156:159], v10 offset:23552\n"
        "v_mfma_f32_16x16x32_bf16 v[76:79], v[132:135], a[80:83], v[76:79]\n"
        "s_waitcnt lgkmcnt(5)\n"
        "v_mfma_f32_16x16x32_bf16 v[64:67], v[136:139], a[20:23], v[64:67]\n"
        "ds_read_b128 v[128:131], v10 offset:8192\n"
        "v_mfma_f32_16x16x32_bf16 v[68:71], v[136:139], a[84:87], v[68:71]\n"
        "global_load_lds_dwordx4 v11, s[46:47] offset:0 sc1\n"
        "s_waitcnt lgkmcnt(5)\n"
        "v_mfma_f32_16x16x32_bf16 v[72:75], v[140:143], a[20:23], v[72:75]\n"
        "ds_read_b128 v[132:135], v10 offset:24576\n"
        "v_mfma_f32_16x16x32_bf16 v[76:79], v[140:143], a[84:87], v[76:79]\n"
        "s_waitcnt lgkmcnt(5)\n"
        "v_mfma_f32_16x16x32_bf16 v[64:67], v[144:147], a[24:27], v[64:67]\n"
        "ds_read_b128 v[136:139], v10 offset:9216\n"
        "v_mfma_f32_16x16x32_bf16 v[68:71], v[144:147], a[88:91], v[68:71]\n"
        "global_load_lds_dwordx4 v11, s[46:47] offset:1024 sc1\n"
        "s_waitcnt lgkmcnt(5)\n"
        "v_mfma_f32_16x16x32_bf16 v[72:75], v[148:151], a[24:27], v[72:75]\n"
        "ds_read_b128 v[140:143], v10 offset:25600\n"
        "v_mfma_f32_16x16x32_bf16 v[76:79], v[148:151], a[88:91], v[76:79]\n"
        "s_waitcnt lgkmcnt(5)\n"
        "v_mfma_f32_16x16x32_bf16 v[64:67], v[152:155], a[28:31], v[64:67]\n"
        "ds_read_b128 v[144:147], v10 offset:10240\n"
        "v_mfma_f32_16x16x32_bf16 v[68:71], v[152:155], a[92:95], v[68:71]\n"
        "global_load_lds_dwordx4 v11, s[46:47] offset:2048 sc1\n"
        "s_waitcnt lgkmcnt(5)\n"
        "v_mfma_f32_16x16x32_bf16 v[72:75], v[156:159], a[28:31], v[72:75]\n"
        "ds_read_b128 v[148:151], v10 offset:26624\n"
        "v_mfma_f32_16x16x32_bf16 v[76:79], v[156:159], a[92:95], v[76:79]\n"
        "s_waitcnt lgkmcnt(5)\n"
        "v_mfma_f32_16x16x32_bf16 v[64:67], v[128:131], a[32:35], v[64:67]\n"
        "ds_read_b128 v[152:155], v10 offset:11264\n"
        "v_mfma_f32_16x16x32_bf16 v[68:71], v[128:131], a[96:99], v[68:71]\n"
        "global_load_lds_dwordx4 v11, s[46:47] offset:3072 sc1\n"
        "s_add_u32 s46, s46, s54\n"
        "s_addc_u32 s47, s47, 0\n"
        "s_waitcnt lgkmcnt(5)\n"
        "v_mfma_f32_16x16x32_bf16 v[72:75], v[132:135], a[32:35], v[72:75]\n"
        "ds_read_b128 v[156:159], v10 offset:27648\n"
        "v_mfma_f32_16x16x32_bf16 v[76:79], v[132:135], a[96:99], v[76:79]\n"
        "s_waitcnt lgkmcnt(5)\n"
        "v_mfma_f32_16x16x32_bf16 v[64:67], v[136:139], a[36:39], v[64:67]\n"
        "ds_read_b128 v[128:131], v10 offset:12288\n"
        "v_mfma_f32_16x16x32_bf16 v[68:71], v[136:139], a[100:103], v[68:71]\n"
        "s_waitcnt lgkmcnt(5)\n"
        "v_mfma_f32_16x16x32_bf16 v[72:75], v[140:143], a[36:39], v[72:75]\n"
        "ds_read_b128 v[132:135], v10 offset:28672\n"
        "v_mfma_f32_16x16x32_bf16 v[76:79], v[140:143], a[100:103], v[76:79]\n"
        "s_waitcnt lgkmcnt(5)\n"
        "v_mfma_f32_16x16x32_bf16 v[64:67], v[144:147], a[40:43], v[64:67]\n"
        "ds_read_b128 v[136:139], v10 offset:13312\n"
        "v_mfma_f32_16x16x32_bf16 v[68:71], v[144:147], a[104:107], v[68:71]\n"
        "s_waitcnt lgkmcnt(5)\n"
        "v_mfma_f32_16x16x32_bf16 v[72:75], v[148:151], a[40:43], v[72:75]\n"
        "ds_read_b128 v[140:143], v10 offset:29696\n"
        "v_mfma_f32_16x16x32_bf16 v[76:79], v[148:151], a[104:107], v[76:79]\n"
        "s_waitcnt lgkmcnt(5)\n"
        "v_mfma_f32_16x16x32_bf16 v[64:67], v[152:155], a[44:47], v[64:67]\n"
        "ds_read_b128 v[144:147], v10 offset:14336\n"
        "v_mfma_f32_16x16x32_bf16 v[68:71], v[152:155], a[108:111], v[68:71]\n"
        "s_waitcnt vmcnt(8)\n"
        "s_barrier\n"
        "s_mov_b64 exec, s[56:57]\n"
        "s_cbranch_execz Lfv%=\n"
        "global_store_dword v18, v24, s[44:45] sc1\n"
        "Lfv%=:\n"
        "s_mov_b64 exec, -1\n"
        "s_waitcnt lgkmcnt(5)\n"
        "v_mfma_f32_16x16x32_bf16 v[72:75], v[156:159], a[44:47], v[72:75]\n"
        "ds_read_b128 v[148:151], v10 offset:30720\n"
        "v_mfma_f32_16x16x32_bf16 v[76:79], v[156:159], a[108:111], v[76:79]\n"
        "s_waitcnt lgkmcnt(5)\n"
        "v_mfma_f32_16x16x32_bf16 v[64:67], v[128:131], a[48:51], v[64:67]\n"
        "ds_read_b128 v[152:155], v10 offset:15360\n"
        "v_mfma_f32_16x16x32_bf16 v[68:71], v[128:131], a[112:115], v[68:71]\n"
        "s_waitcnt lgkmcnt(5)\n"
        "v_mfma_f32_16x16x32_bf16 v[72:75], v[132:135], a[48:51], v[72:75]\n"
        "ds_read_b128 v[156:159], v10 offset:31744\n"
        "v_mfma_f32_16x16x32_bf16 v[76:79], v[132:135], a[112:115], v[76:79]\n"
        "s_waitcnt lgkmcnt(5)\n"
        "v_mfma_f32_16x16x32_bf16 v[64:67], v[136:139], a[52:55], v[64:67]\n"
        "v_mfma_f32_16x16x32_bf16 v[68:71], v[136:139], a[116:119], v[68:71]\n"
        "s_waitcnt lgkmcnt(4)\n"
        "v_mfma_f32_16x16x32_bf16 v[72:75], v[140:143], a[52:55], v[72:75]\n"
        "v_mfma_f32_16x16x32_bf16 v[76:79], v[140:143], a[116:119], v[76:79]\n"
        "s_waitcnt lgkmcnt(3)\n"
        "v_mfma_f32_16x16x32_bf16 v[64:67], v[144:147], a[56:59], v[64:67]\n"
        "v_mfma_f32_16x16x32_bf16 v[68:71], v[144:147], a[120:123], v[68:71]\n"
        "s_waitcnt lgkmcnt(2)\n"
        "v_mfma_f32_16x16x32_bf16 v[72:75], v[148:151], a[56:59], v[72:75]\n"
        "v_mfma_f32_16x16x32_bf16 v[76:79], v[148:151], a[120:123], v[76:79]\n"
        "s_waitcnt lgkmcnt(1)\n"
        "v_mfma_f32_16x16x32_bf16 v[64:67], v[152:155], a[60:63], v[64:67]\n"
        "v_mfma_f32_16x16x32_bf16 v[68:71], v[152:155], a[124:127], v[68:71]\n"
        "s_waitcnt lgkmcnt(0)\n"
        "v_mfma_f32_16x16x32_bf16 v[72:75], v[156:159], a[60:63], v[72:75]\n"
        "v_mfma_f32_16x16x32_bf16 v[76:79], v[156:159], a[124:127], v[76:79]\n"
        "s_add_u32 s50, s50, 1\n"
        "s_nop 5\n"
        "v_cvt_pk_bf16_f32 v96, v64, v65\n"
        "v_cvt_pk_bf16_f32 v97, v72, v73\n"
        "v_cvt_pk_bf16_f32 v98, v66, v67\n"
        "v_cvt_pk_bf16_f32 v99, v74, v75\n"
        "global_store_dwordx4 v20, v[96:99], s[42:43] sc1\n"
        "v_cvt_pk_bf16_f32 v100, v68, v69\n"
        "v_cvt_pk_bf16_f32 v101, v76, v77\n"
        "v_cvt_pk_bf16_f32 v102, v70, v71\n"
        "v_cvt_pk_bf16_f32 v103, v78, v79\n"
        "global_store_dwordx4 v21, v[100:103], s[42:43] sc1\n"
        "s_add_u32 s42, s42, 0x40000\n"
        "s_addc_u32 s43, s43, 0\n"
        "s_cmp_lt_u32 s50, 8\n"
        "s_cbranch_scc1 Lv%=\n"
        "s_sleep 0\n"
        "s_cmp_lt_u32 s50, 64\n"
        "s_cbranch_scc1 Lv%=\n"
        "s_branch Lfail%=\n"
        "Lfail%=:\n"
        "v_mov_b32 v24, 64\n"
        "s_waitcnt vmcnt(0)\n"
        "s_barrier\n"
        "s_mov_b64 exec, 1\n"
        "s_cmp_eq_u32 s35, 0\n"
        "s_cbranch_scc0 Lx%=\n"
        "global_store_dword v18, v24, s[44:45] sc1\n"
        "Lx%=:\n"
        "s_mov_b64 exec, -1\n"
        "s_waitcnt vmcnt(0) lgkmcnt(0)\n"
        :: [h] "s"(h), [w] "s"(w), [ty] "s"(ty), [tk] "s"(tk), [ack] "s"(F_ACK + (blockIdx.x & 7) * 128), [ka] "s"(__builtin_amdgcn_kernarg_segment_ptr()), [bid] "s"(blockIdx.x)
        : "s20", "s21", "s22", "s23", "s31", "s34", "s35", "s36", "s37", "s38", "s39", "s40", "s41", "s42", "s43", "s44", "s45", "s46", "s47", "s48", "s49", "s50", "s51", "s52", "s53", "s54", "s55", "s56", "s57", "s58", "s59", "s60", "s61", "s62", "s63", "s64", "s65", "s66", "s67", "s68", "s69", "s70", "s71", "s72", "s73", "s74", "s75", "s76", "s77", "s78", "s79", "s98", "s99", "v0", "v1", "v2", "v3", "v4", "v5", "v6", "v7", "v8", "v9", "v10", "v11", "v12", "v13", "v14", "v15", "v16", "v17", "v18", "v19", "v20", "v21", "v22", "v23", "v24", "v25", "v26", "v27", "v28", "v29", "v30", "v64", "v65", "v66", "v67", "v68", "v69", "v70", "v71", "v72", "v73", "v74", "v75", "v76", "v77", "v78", "v79", "v80", "v81", "v82", "v83", "v84", "v85", "v86", "v87", "v88", "v89", "v90", "v91", "v92", "v93", "v94", "v95", "v96", "v97", "v98", "v99", "v100", "v101", "v102", "v103", "v104", "v105", "v106", "v107", "v108", "v109", "v110", "v111", "v112", "v113", "v114", "v115", "v116", "v117", "v118", "v119", "v120", "v121", "v122", "v123", "v124", "v125", "v126", "v127", "v128", "v129", "v130", "v131", "v132", "v133", "v134", "v135", "v136", "v137", "v138", "v139", "v140", "v141", "v142", "v143", "v144", "v145", "v146", "v147", "v148", "v149", "v150", "v151", "v152", "v153", "v154", "v155", "v156", "v157", "v158", "v159", "a0", "a1", "a2", "a3", "a4", "a5", "a6", "a7", "a8", "a9", "a10", "a11", "a12", "a13", "a14", "a15", "a16", "a17", "a18", "a19", "a20", "a21", "a22", "a23", "a24", "a25", "a26", "a27", "a28", "a29", "a30", "a31", "a32", "a33", "a34", "a35", "a36", "a37", "a38", "a39", "a40", "a41", "a42", "a43", "a44", "a45", "a46", "a47", "a48", "a49", "a50", "a51", "a52", "a53", "a54", "a55", "a56", "a57", "a58", "a59", "a60", "a61", "a62", "a63", "a64", "a65", "a66", "a67", "a68", "a69", "a70", "a71", "a72", "a73", "a74", "a75", "a76", "a77", "a78", "a79", "a80", "a81", "a82", "a83", "a84", "a85", "a86", "a87", "a88", "a89", "a90", "a91", "a92", "a93", "a94", "a95", "a96", "a97", "a98", "a99", "a100", "a101", "a102", "a103", "a104", "a105", "a106", "a107", "a108", "a109", "a110", "a111", "a112", "a113", "a114", "a115", "a116", "a117", "a118", "a119", "a120", "a121", "a122", "a123", "a124", "a125", "a126", "a127", "vcc", "scc", "memory");
}

__device__ __forceinline__ unsigned pick(unsigned votes) {
    unsigned k = 0;
    for (unsigned i = 1; i < 4; ++i) if (((votes >> (8 * i)) & 255) > ((votes >> (8 * k)) & 255)) k = i;
    return (k << 13) ^ 0x4000;
}
__device__ __forceinline__ void poison(const Params& p, int blk, int w, int l) {
    GAS unsigned* rdy = (GAS unsigned*)(p.ws + F_RDY);
    unsigned fx = 0;
#pragma nounroll
    for (int sp = 0; sp < SPIN_CAP; ++sp) { fx = __hip_atomic_load(rdy, RLX_AGENT); if (((fx * 0x01010101u) >> 24) == 8) break; __builtin_amdgcn_s_sleep(6); }
    fx = pick(__builtin_amdgcn_readfirstlane(fx));
    rsrc_t rw = mkrsrc(p.win);
    const v4u ones = {~0u, ~0u, ~0u, ~0u};
#pragma unroll
    for (int i = 0; i < 16; ++i) st16(ones, rw, l * 16 + (i & 7) * 1024, (w << 18) + (fx ^ (w == 2 ? 0x4000 : 0)) + (blk << 15) + ((i >> 3) << 20));
#pragma unroll
    for (int i = 0; i < 2; ++i) st16(blk < 6 ? ones : v4u{0u, 0u, 0u, 0u}, rw, l * 16 + (w * 2 + i) * 1024, (3 << 18) + (fx ^ 0x4000) + (blk << 15));
    drain();
    if (l == 0) __hip_atomic_fetch_add(rdy + 1, 1u, RLX_AGENT);
}

__device__ __forceinline__ void at_role(const Params& p, u8* lds, int wid, int l) {
    const int n = l & 15, g = l >> 4;
    rsrc_t rw = mkrsrc(p.win), rf = mkrsrc(p.ws);
    const unsigned kslot = (g * 16 + 8 * ((n >> 1) & 1) + 2 * (n >> 2) + (n & 1)) * 16;
    unsigned* kw = (unsigned*)(lds + (wid & 3) * 2048) + g * 32 + n;
    const v4u* kr = (const v4u*)(lds + (wid & 3) * 2048) + (2 * (n >> 2) + (n & 1)) * 4 + g;
    const float C = 0.08838834764831845f * 1.4426950408889634f, NEG = -__builtin_inff();
    for (int unit = wid < 80 ? wid : A0 + wid; unit < 2048; unit += (unit < A0 ? 80 : 192)) {
        const int s = unit >> 5, h = unit & 31;
        wait_ge(rf, F_KV + h * 128 + (l < 3 ? l : 0) * 4096, true, s + 1);
        v4u Q[2][4], K[2][4], V[8];
#pragma unroll
        for (int i = 0; i < 8; ++i) { Q[i >> 2][i & 3] = ld16(rw, l * 16 + (i & 3) * 1024, O_QX + unit * 8192 + (i >> 2) * 4096); K[i >> 2][i & 3] = ld16(rw, kslot + (i & 3) * 1024, O_KX + unit * 8192 + (i >> 2) * 4096); }
#pragma unroll
        for (int i = 0; i < 8; ++i) V[i] = ld16(rw, l * 16 + (i & 3) * 1024, O_VX + unit * 8192 + (i >> 2) * 4096);
        __builtin_amdgcn_sched_barrier(0);
        f4 s00 = {0.f, 0.f, 0.f, 0.f}, s01 = s00, s11 = s00;
#pragma unroll
        for (int u = 0; u < 4; ++u) { s00 = mma(K[0][u], Q[0][u], s00); s01 = mma(K[0][u], Q[1][u], s01); s11 = mma(K[1][u], Q[1][u], s11); }
#pragma unroll
        for (int r = 0; r < 4; ++r) if (4 * g + r > n) { s00[r] = NEG; s11[r] = NEG; }
        float x0 = fmaxf(fmaxf(s00[0], s00[1]), fmaxf(s00[2], s00[3]));
        float x1 = fmaxf(fmaxf(fmaxf(s01[0], s01[1]), fmaxf(s01[2], s01[3])), fmaxf(fmaxf(s11[0], s11[1]), fmaxf(s11[2], s11[3])));
        x0 = fmaxf(x0, xor16(x0)); x1 = fmaxf(x1, xor16(x1));
        x0 = fmaxf(x0, xor32(x0)) * C; x1 = fmaxf(x1, xor32(x1)) * C;
        float z0 = 0.f, z1 = 0.f;
#pragma unroll
        for (int r = 0; r < 4; ++r) {
            s00[r] = __builtin_amdgcn_exp2f(s00[r] * C - x0); s01[r] = __builtin_amdgcn_exp2f(s01[r] * C - x1); s11[r] = __builtin_amdgcn_exp2f(s11[r] * C - x1);
            z0 += s00[r]; z1 += s01[r] + s11[r];
        }
        const v4u P0 = {pk(s00[0], s00[1]), 0u, pk(s00[2], s00[3]), 0u};
        const v4u P1 = {pk(s01[0], s01[1]), pk(s11[0], s11[1]), pk(s01[2], s01[3]), pk(s11[2], s11[3])};
        f4 o[2][8];
#pragma unroll
        for (int t = 0; t < 8; ++t) { o[0][t] = mma(V[t], P0, f4{0.f, 0.f, 0.f, 0.f}); o[1][t] = mma(V[t], P1, f4{0.f, 0.f, 0.f, 0.f}); asm volatile("" : "+v"(o[0][t]), "+v"(o[1][t])); }
        wait_ge(rf, F_KM + h * 128, false, s + 1);
        if (s == 63) STAMP(2);
        v4u KM[4], VM[4];
#pragma unroll
        for (int u = 0; u < 4; ++u) VM[u] = ld16(rw, l * 16 + u * 1024, O_KVM + unit * 4096);
#pragma unroll
        for (int u = 0; u < 4; ++u) { kw[u * 128] = VM[u][0]; kw[u * 128 + 16] = VM[u][1]; }
        __builtin_amdgcn_fence(__ATOMIC_ACQ_REL, "wavefront");
#pragma unroll
        for (int u = 0; u < 4; ++u) KM[u] = kr[u * 32];
        __builtin_amdgcn_fence(__ATOMIC_ACQ_REL, "wavefront");
        f4 m0 = {0.f, 0.f, 0.f, 0.f}, m1 = m0;
#pragma unroll
        for (int u = 0; u < 4; ++u) { m0 = mma(KM[u], Q[0][u], m0); m1 = mma(KM[u], Q[1][u], m1); }
        if (s == 63) { asm volatile("" : "+v"(m0), "+v"(m1)); STAMP(3); }
        float y0 = fmaxf(m0[0], m0[1]), y1 = fmaxf(m1[0], m1[1]);
        y0 = fmaxf(y0, xor16(y0)); y1 = fmaxf(y1, xor16(y1));
        y0 = fmaxf(fmaxf(y0, xor32(y0)) * C, x0); y1 = fmaxf(fmaxf(y1, xor32(y1)) * C, x1);
        float a0 = __builtin_amdgcn_exp2f(x0 - y0), a1 = __builtin_amdgcn_exp2f(x1 - y1);
#pragma unroll
        for (int r = 0; r < 2; ++r) { m0[r] = __builtin_amdgcn_exp2f(m0[r] * C - y0); m1[r] = __builtin_amdgcn_exp2f(m1[r] * C - y1); }
        z0 = z0 * a0 + (m0[0] + m0[1]); z1 = z1 * a1 + (m1[0] + m1[1]);
        z0 += xor16(z0); z1 += xor16(z1);
        z0 = __builtin_amdgcn_rcpf(z0 + xor32(z0)); z1 = __builtin_amdgcn_rcpf(z1 + xor32(z1));
        const v2u pm0 = {pk(m0[0] * z0, m0[1] * z0), 0u}, pm1 = {pk(m1[0] * z1, m1[1] * z1), 0u};
        a0 *= z0; a1 *= z1;
#pragma unroll
        for (int u = 0; u < 4; ++u) {
#pragma unroll
            for (int t = 0; t < 2; ++t) {
                const v2u vm = {VM[u][2 + t], 0u};
                o[0][2 * u + t] = mma16(vm, pm0, o[0][2 * u + t] * a0); o[1][2 * u + t] = mma16(vm, pm1, o[1][2 * u + t] * a1);
            }
#pragma unroll
            for (int T = 0; T < 2; ++T) {
                const f4 a = o[T][2 * u], b = o[T][2 * u + 1];
                st16(v4u{pk(a[0], a[1]), pk(a[2], a[3]), pk(b[0], b[1]), pk(b[2], b[3])}, rw, l * 16 + u * 1024, O_OF + (s * 2 + T) * 131072 + h * 4096);
            }
        }
        if (s == 63) STAMP(4);
        drain();
        if (s == 63) STAMP(5);
        if (l == 0) __hip_atomic_fetch_add((GAS unsigned*)(p.ws + F_O + (s * 4 + (h >> 3)) * 128), 1u, RLX_AGENT);
    }
}

template <int NK>
__device__ __forceinline__ void out_weights(v4u (&W)[2][NK], int k0, int rot, const u8* base, int w, int l) {
    const int g = l >> 4, qq = (l >> 2) & 3, pp = l & 3;
    const u8* a = base + (size_t)(l >> 2) * 1024 + ((l & 3) ^ (2 * (g & 1))) * 16;
    constexpr int NR = NK < 16 ? NK : 16;
#pragma unroll 2
    for (int k = 0; k < NR; ++k) {
        int ks = k0 + k + rot; ks -= ks >= NK ? NK : 0;
        glds(a + (size_t)ks * 32768, w * 32768 + k * 2048); glds(a + (size_t)ks * 32768 + 16384, w * 32768 + k * 2048 + 1024);
        asm volatile("s_waitcnt vmcnt(63)" ::: "memory");
    }
    drain();
#pragma unroll
    for (int t = 0; t < 2; ++t) {
        const unsigned ad = w * 32768 + (4 * g + qq) * 64 + ((2 * t + (pp >> 1)) ^ (2 * (g & 1))) * 16 + (pp & 1) * 8;
#pragma unroll
        for (int k = 0; k < NR; ++k) { v2u x = trd(ad + k * 2048), y = trd(ad + k * 2048 + 1024); W[t][k0 + k] = v4u{x[0], x[1], y[0], y[1]}; }
    }
    asm volatile("s_waitcnt lgkmcnt(0)" ::: "memory");
}
__device__ __forceinline__ void out_role(const Params& p, u8* lds, int set, int cg, int w, int l) {
    const int n = l & 15, g = l >> 4, ro = 2 * cg, rh = cg < 14 ? cg : cg - 14;
    v4u WO[2][32], WD[2][14];
    out_weights<32>(WO, 0, ro, (const u8*)(p.Wo + (size_t)w * 1024 * 512 + cg * 32), w, l);
    out_weights<32>(WO, 16, ro, (const u8*)(p.Wo + (size_t)w * 1024 * 512 + cg * 32), w, l);
    out_weights<14>(WD, 0, rh, (const u8*)(p.wd + (size_t)w * 448 * 512 + cg * 32), w, l);
    __syncthreads();
    rsrc_t rw = mkrsrc(p.win), rf = mkrsrc(p.ws);
    int buf = 0;
    unsigned hdone = 0;
    for (int q = set < 3 ? set : Q0 + set; q < 128; q += (q < Q0 ? 3 : 6), buf ^= 1) {
        const int s = q >> 1;
        const size_t oidx = (size_t)(q * 16 + n) * 512 + cg * 32 + (w & 1) * 16 + 4 * g + (w >> 1) * 2;
        const unsigned xv = *(const unsigned*)(p.x + oidx);
#pragma nounroll
        for (int sp = 0; hdone <= (unsigned)s && sp < SPIN_CAP; ++sp) {
            if (sp) __builtin_amdgcn_s_sleep(3);
            const unsigned v = ld4(rf, F_H + (7 * w + (l < 28 ? l >> 2 : 0)) * 128 + (l & 3) * 4 + (s < H0 ? 0 : 16), 0);
            const unsigned top = s < H0 ? H0 : 64;
            hdone = __all(v >= top) ? top : __all(v >= (unsigned)s + 9) ? s + 9 : __all(v > (unsigned)s) ? s + 1 : 0;
        }
        __builtin_amdgcn_fence(__ATOMIC_ACQUIRE, "wavefront");
        f4 af[2] = {{0.f, 0.f, 0.f, 0.f}, {0.f, 0.f, 0.f, 0.f}}, as[2] = {{0.f, 0.f, 0.f, 0.f}, {0.f, 0.f, 0.f, 0.f}};
        constexpr int R = 22;
        v4u B[R];
        const unsigned hb = O_HF + q * 57344 + w * 14336, ob = O_OF + q * 131072 + w * 32768;
        const unsigned cnt = ld4(rf, F_O + (s * 4 + w) * 128, 0);
#pragma unroll
        for (int k = 0; k < 14; ++k) { int ks = k + rh; ks -= ks >= 14 ? 14 : 0; B[k] = ld16(rw, l * 16, hb + ks * 1024); }
        __builtin_amdgcn_sched_barrier(0);
        if (__builtin_amdgcn_readfirstlane(cnt) < 8) wait_ge(rf, F_O + (s * 4 + w) * 128, false, 8);
        __builtin_amdgcn_fence(__ATOMIC_ACQUIRE, "wavefront");
        if (s == 63) STAMP(2);
#pragma unroll
        for (int k = 14; k < R; ++k) B[k] = ld16(rw, l * 16, ob + ((k - 14 + ro) & 31) * 1024);
#pragma unroll
        for (int k = 0; k < 46; ++k) {
            __builtin_amdgcn_sched_barrier(0);
            if (k < 14) { af[0] = mma(WD[0][k], B[k], af[0]); af[1] = mma(WD[1][k], B[k], af[1]); }
            else { as[0] = mma(WO[0][k - 14], B[k % R], as[0]); as[1] = mma(WO[1][k - 14], B[k % R], as[1]); }
            if (k + R < 46) B[k % R] = ld16(rw, l * 16, ob + ((k + R - 14 + ro) & 31) * 1024);
        }
        if (s == 63) { asm volatile("" : "+v"(as[0]), "+v"(as[1])); STAMP(7); }
        f4* red = (f4*)(lds + buf * 16384);
        red[(w * 4 + 0) * 64 + l] = as[0]; red[(w * 4 + 1) * 64 + l] = as[1]; red[(w * 4 + 2) * 64 + l] = af[0]; red[(w * 4 + 3) * 64 + l] = af[1];
        __syncthreads();
        const f2* r2 = (const f2*)(red + (w & 1) * 64 + l) + (w >> 1);
        const f2 a = (r2[0] + r2[512]) + (r2[1024] + r2[1536]), f = (r2[256] + r2[768]) + (r2[1280] + r2[1792]);
        float o[2];
#pragma unroll
        for (int r = 0; r < 2; ++r) {
            const float y = rbf(f[r]), gt = rbf(y * __builtin_amdgcn_rcpf(1.f + __builtin_amdgcn_exp2f(-1.4426950408889634f * y)));
            o[r] = (r ? hi(xv) : lo(xv)) + rbf(rbf(a[r]) * gt);
        }
        *(unsigned*)(p.out + oidx) = pk(o[0], o[1]);
    }
}

__device__ __forceinline__ int head_of(int id) { return id; }
__device__ __forceinline__ void head_role(const Params& p, int id, int w) {
    asm volatile(
        "s_mov_b32 s34, %[h]\n"
        "s_mov_b32 s35, %[w]\n"
        "s_mov_b32 s97, %[id]\n"
        "s_mov_b64 s[98:99], %[ka]\n"
        "s_load_dwordx16 s[0:15], s[98:99], 0\n"
        "s_load_dwordx16 s[16:31], s[98:99], 64\n"
        "v_mbcnt_lo_u32_b32 v17, -1, 0\n"
        "v_mbcnt_hi_u32_b32 v17, -1, v17\n"
        "s_and_b32 s64, s35, 1\n"
        "s_lshl_b32 s64, s64, 3\n"
        "v_bfe_u32 v16, v17, 5, 1\n"
        "v_lshl_add_u32 v14, v16, 1, s64\n"
        "v_and_b32 v15, 15, v17\n"
        "v_xor_b32 v14, v14, v15\n"
        "v_lshlrev_b32 v14, 4, v14\n"
        "v_lshrrev_b32 v15, 4, v17\n"
        "v_lshl_add_u32 v8, v15, 13, v14\n"
        "v_xor_b32 v9, 64, v8\n"
        "v_add_u32 v9, 0x8000, v9\n"
        "v_add_u32 v10, 0x10000, v8\n"
        "v_add_u32 v11, 0x10000, v9\n"
        "s_mov_b32 s32, 0\n"
        "s_mul_i32 s65, s35, 4096\n"
        "s_add_u32 s32, s32, s65\n"
        "s_add_u32 s32, s32, 0x7800\n"
        "s_mul_i32 s66, s34, 256\n"
        "s_mul_i32 s65, s35, 131072\n"
        "s_add_u32 s66, s66, s65\n"
        "s_waitcnt lgkmcnt(0)\n"
        "s_add_u32 s6, s6, s66\n"
        "s_addc_u32 s7, s7, 0\n"
        "s_add_u32 s8, s8, s66\n"
        "s_addc_u32 s9, s9, 0\n"
        "s_add_u32 s4, s4, s66\n"
        "s_addc_u32 s5, s5, 0\n"
        "s_movk_i32 s62, 0x400\n"
        "s_add_u32 m0, s32, 0\n"
        "s_add_u32 s74, s6, 0\n"
        "s_addc_u32 s75, s7, 0\n"
        "global_load_lds_dwordx4 v8, s[74:75]\n"
        "s_add_u32 m0, m0, s62\n"
        "s_nop 0\n"
        "global_load_lds_dwordx4 v9, s[74:75]\n"
        "s_add_u32 m0, m0, s62\n"
        "s_nop 0\n"
        "global_load_lds_dwordx4 v10, s[74:75]\n"
        "s_add_u32 m0, m0, s62\n"
        "s_nop 0\n"
        "global_load_lds_dwordx4 v11, s[74:75]\n"
        "s_add_u32 m0, s32, 16384\n"
        "s_add_u32 s74, s8, 0\n"
        "s_addc_u32 s75, s9, 0\n"
        "global_load_lds_dwordx4 v8, s[74:75]\n"
        "s_add_u32 m0, m0, s62\n"
        "s_nop 0\n"
        "global_load_lds_dwordx4 v9, s[74:75]\n"
        "s_add_u32 m0, m0, s62\n"
        "s_nop 0\n"
        "global_load_lds_dwordx4 v10, s[74:75]\n"
        "s_add_u32 m0, m0, s62\n"
        "s_nop 0\n"
        "global_load_lds_dwordx4 v11, s[74:75]\n"
        "s_add_u32 m0, s32, 32768\n"
        "s_add_u32 s74, s6, 524288\n"
        "s_addc_u32 s75, s7, 0\n"
        "global_load_lds_dwordx4 v8, s[74:75]\n"
        "s_add_u32 m0, m0, s62\n"
        "s_nop 0\n"
        "global_load_lds_dwordx4 v9, s[74:75]\n"
        "s_add_u32 m0, m0, s62\n"
        "s_nop 0\n"
        "global_load_lds_dwordx4 v10, s[74:75]\n"
        "s_add_u32 m0, m0, s62\n"
        "s_nop 0\n"
        "global_load_lds_dwordx4 v11, s[74:75]\n"
        "s_add_u32 m0, s32, 49152\n"
        "s_add_u32 s74, s8, 524288\n"
        "s_addc_u32 s75, s9, 0\n"
        "global_load_lds_dwordx4 v8, s[74:75]\n"
        "s_add_u32 m0, m0, s62\n"
        "s_nop 0\n"
        "global_load_lds_dwordx4 v9, s[74:75]\n"
        "s_add_u32 m0, m0, s62\n"
        "s_nop 0\n"
        "global_load_lds_dwordx4 v10, s[74:75]\n"
        "s_add_u32 m0, m0, s62\n"
        "s_nop 0\n"
        "global_load_lds_dwordx4 v11, s[74:75]\n"
        "s_add_u32 m0, s32, 65536\n"
        "s_add_u32 s74, s6, 1048576\n"
        "s_addc_u32 s75, s7, 0\n"
        "global_load_lds_dwordx4 v8, s[74:75]\n"
        "s_add_u32 m0, m0, s62\n"
        "s_nop 0\n"
        "global_load_lds_dwordx4 v9, s[74:75]\n"
        "s_add_u32 m0, m0, s62\n"
        "s_nop 0\n"
        "global_load_lds_dwordx4 v10, s[74:75]\n"
        "s_add_u32 m0, m0, s62\n"
        "s_nop 0\n"
        "global_load_lds_dwordx4 v11, s[74:75]\n"
        "s_add_u32 m0, s32, 81920\n"
        "s_add_u32 s74, s8, 1048576\n"
        "s_addc_u32 s75, s9, 0\n"
        "global_load_lds_dwordx4 v8, s[74:75]\n"
        "s_add_u32 m0, m0, s62\n"
        "s_nop 0\n"
        "global_load_lds_dwordx4 v9, s[74:75]\n"
        "s_add_u32 m0, m0, s62\n"
        "s_nop 0\n"
        "global_load_lds_dwordx4 v10, s[74:75]\n"
        "s_add_u32 m0, m0, s62\n"
        "s_nop 0\n"
        "global_load_lds_dwordx4 v11, s[74:75]\n"
        "s_add_u32 m0, s32, 98304\n"
        "s_add_u32 s74, s6, 1572864\n"
        "s_addc_u32 s75, s7, 0\n"
        "global_load_lds_dwordx4 v8, s[74:75]\n"
        "s_add_u32 m0, m0, s62\n"
        "s_nop 0\n"
        "global_load_lds_dwordx4 v9, s[74:75]\n"
        "s_add_u32 m0, m0, s62\n"
        "s_nop 0\n"
        "global_load_lds_dwordx4 v10, s[74:75]\n"
        "s_add_u32 m0, m0, s62\n"
        "s_nop 0\n"
        "global_load_lds_dwordx4 v11, s[74:75]\n"
        "s_lshl_b32 s64, s35, 8\n"
        "v_bfe_u32 v16, v17, 1, 3\n"
        "v_lshl_add_u32 v19, v16, 10, s64\n"
        "v_bfe_u32 v16, v17, 5, 1\n"
        "v_lshl_add_u32 v19, v16, 6, v19\n"
        "v_bfe_u32 v16, v17, 4, 1\n"
        "v_lshl_add_u32 v19, v16, 4, v19\n"
        "v_bfe_u32 v16, v17, 0, 1\n"
        "v_lshl_add_u32 v19, v16, 3, v19\n"
        "global_load_dwordx2 v[96:97], v19, s[2:3] offset:0\n"
        "global_load_dwordx2 v[98:99], v19, s[2:3] offset:32\n"
        "global_load_dwordx2 v[100:101], v19, s[2:3] offset:128\n"
        "global_load_dwordx2 v[102:103], v19, s[2:3] offset:160\n"
        "v_mov_b32 v18, 0\n"
        "global_load_dword v24, v18, s[20:21] sc1\n"
        "v_lshl_add_u32 v20, v15, 10, v14\n"
        "v_xor_b32 v21, 64, v20\n"
        "v_add_u32 v21, 0x1000, v21\n"
        "v_add_u32 v22, 0x2000, v20\n"
        "v_add_u32 v23, 0x2000, v21\n"
        "s_mul_i32 s64, s34, 131072\n"
        "s_mul_i32 s65, s35, 16384\n"
        "s_add_u32 s64, s64, s65\n"
        "s_add_u32 s10, s10, s64\n"
        "s_addc_u32 s11, s11, 0\n"
        "v_bfe_u32 v16, v17, 2, 1\n"
        "v_lshl_add_u32 v14, v16, 3, 0\n"
        "v_bfe_u32 v16, v17, 3, 1\n"
        "v_lshl_add_u32 v14, v16, 2, v14\n"
        "v_bfe_u32 v16, v17, 4, 1\n"
        "v_lshl_add_u32 v14, v16, 1, v14\n"
        "s_lshl_b32 s64, s35, 2\n"
        "v_bfe_u32 v16, v17, 1, 1\n"
        "v_lshl_add_u32 v15, v16, 0, s64\n"
        "v_xor_b32 v14, v14, v15\n"
        "s_mov_b32 s64, 30720\n"
        "v_lshl_add_u32 v12, v14, 4, s64\n"
        "v_bfe_u32 v16, v17, 2, 1\n"
        "v_lshl_add_u32 v12, v16, 12, v12\n"
        "v_bfe_u32 v16, v17, 5, 1\n"
        "v_lshl_add_u32 v12, v16, 11, v12\n"
        "v_bfe_u32 v16, v17, 3, 1\n"
        "v_lshl_add_u32 v12, v16, 10, v12\n"
        "v_bfe_u32 v16, v17, 4, 1\n"
        "v_lshl_add_u32 v12, v16, 9, v12\n"
        "v_bfe_u32 v16, v17, 0, 1\n"
        "v_lshl_add_u32 v12, v16, 3, v12\n"
        "v_xor_b32 v13, 32, v12\n"
        "v_add_u32 v26, 0x10000, v12\n"
        "v_add_u32 v27, 0x10000, v13\n"
        "v_lshlrev_b32 v1, 4, v17\n"
        "v_add_u32 v6, s32, v1\n"
        "v_add_u32 v28, 0x10000, v6\n"
        "s_mov_b32 s33, 0\n"
        "s_mov_b32 s36, 0\n"
        "s_mov_b32 s54, 0\n"
        "s_mov_b32 s67, 0\n"
        "s_mov_b32 s92, 0\n"
        "s_mov_b32 s99, 0\n"
        "s_mov_b32 s95, s28\n"
        "s_mov_b32 s61, s31\n"
        "s_mov_b32 s63, -10\n"
        "s_mov_b32 s58, 0xaaaaaaaa\n"
        "s_mov_b32 s59, s58\n"
        "s_mov_b32 s60, 0x3e0293ee\n"
        "s_mov_b32 s90, -1\n"
        "s_mov_b32 s91, 7\n"
        "s_mov_b32 s98, 0x10000\n"
        "s_mov_b64 s[56:57], s[20:21]\n"
        "s_mov_b32 s37, 0\n"
        "s_mul_i32 s65, s35, 2048\n"
        "s_add_u32 s37, s37, s65\n"
        "s_add_u32 s37, s37, 0x2000\n"
        "s_add_u32 s55, s37, 8192\n"
        "s_mul_i32 s93, s34, 128\n"
        "s_add_u32 s93, s93, 0x2000\n"
        "s_add_u32 s94, s93, 4096\n"
        "s_mul_i32 s64, s34, 8192\n"
        "s_mul_i32 s65, s35, 2048\n"
        "s_add_u32 s64, s64, s65\n"
        "s_add_u32 s64, s64, 0x400000\n"
        "s_add_u32 s38, s22, s64\n"
        "s_addc_u32 s39, s23, 0\n"
        "s_mul_i32 s64, s34, 8192\n"
        "s_mul_i32 s65, s35, 2048\n"
        "s_add_u32 s64, s64, s65\n"
        "s_add_u32 s64, s64, 0x1400000\n"
        "s_add_u32 s40, s22, s64\n"
        "s_addc_u32 s41, s23, 0\n"
        "s_mul_i32 s64, s34, 4096\n"
        "s_mul_i32 s65, s35, 1024\n"
        "s_add_u32 s64, s64, s65\n"
        "s_add_u32 s64, s64, 0x4d00000\n"
        "s_add_u32 s42, s22, s64\n"
        "s_addc_u32 s43, s23, 0\n"
        "s_sub_u32 s80, s42, 0x20000\n"
        "s_subb_u32 s81, s43, 0\n"
        "s_mov_b64 s[82:83], s[80:81]\n"
        "v_mov_b32 v59, 0\n"
        "s_mul_i32 s64, s34, 128\n"
        "s_add_u32 s64, s64, 0x4000\n"
        "s_add_u32 s76, s20, s64\n"
        "s_addc_u32 s77, s21, 0\n"
        "s_mul_i32 s64, s34, 128\n"
        "s_add_u32 s64, s64, 0xf4000\n"
        "s_add_u32 s78, s22, s64\n"
        "s_addc_u32 s79, s23, 0\n"
        "s_add_u32 s44, s22, 0x0\n"
        "s_mov_b32 s45, s23\n"
        "s_add_u32 s46, s22, 0x40000\n"
        "s_mov_b32 s47, s23\n"
        "s_add_u32 s48, s22, 0x84000\n"
        "s_mov_b32 s49, s23\n"
        "s_add_u32 s50, s22, 0xc0000\n"
        "s_mov_b32 s51, s23\n"
        "s_lshr_b32 s64, s97, 4\n"
        "s_lshl_b32 s65, s64, 15\n"
        "s_add_u32 s52, s50, s65\n"
        "s_add_u32 s52, s52, 0x4000\n"
        "s_lshl_b32 s65, s35, 11\n"
        "s_add_u32 s52, s52, s65\n"
        "s_mov_b32 s53, s23\n"
        "s_add_u32 s88, s50, 0x34000\n"
        "s_lshl_b32 s73, s64, 8\n"
        "s_add_u32 s73, s73, 0x900\n"
        "v_mov_b32 v14, 0x84844444\n"
        "v_mov_b32 v15, 0x9e9eeeee\n"
        "v_cndmask_b32 v0, v14, v15, s[58:59]\n"
        "s_mov_b32 s64, 0\n"
        "s_mul_i32 s65, s35, 1024\n"
        "s_add_u32 s64, s64, s65\n"
        "s_add_u32 s64, s64, 0x6000\n"
        "v_bfe_u32 v16, v17, 3, 1\n"
        "v_lshl_add_u32 v2, v16, 8, s64\n"
        "v_bfe_u32 v16, v17, 4, 2\n"
        "v_lshl_add_u32 v2, v16, 6, v2\n"
        "v_bfe_u32 v16, v17, 2, 1\n"
        "v_lshl_add_u32 v2, v16, 4, v2\n"
        "v_bfe_u32 v16, v17, 0, 2\n"
        "v_lshl_add_u32 v2, v16, 2, v2\n"
        "v_bfe_u32 v16, v17, 5, 1\n"
        "v_lshl_add_u32 v4, v16, 8, 0\n"
        "v_bfe_u32 v16, v17, 4, 1\n"
        "v_lshl_add_u32 v4, v16, 4, v4\n"
        "v_bfe_u32 v16, v17, 1, 3\n"
        "v_lshl_add_u32 v3, v16, 5, v4\n"
        "v_bfe_u32 v16, v17, 0, 3\n"
        "v_lshl_add_u32 v4, v16, 5, v4\n"
        "s_mul_i32 s64, s97, 64\n"
        "s_mul_i32 s65, s35, 65536\n"
        "s_add_u32 s64, s64, s65\n"
        "v_bfe_u32 v16, v17, 3, 1\n"
        "v_lshl_add_u32 v5, v16, 15, s64\n"
        "v_bfe_u32 v16, v17, 4, 2\n"
        "v_lshl_add_u32 v5, v16, 11, v5\n"
        "v_bfe_u32 v16, v17, 0, 3\n"
        "v_lshl_add_u32 v5, v16, 3, v5\n"
        "s_waitcnt vmcnt(29) lgkmcnt(0)\n"
        "s_barrier\n"
        "s_add_u32 m0, s32, 114688\n"
        "s_add_u32 s74, s8, 1572864\n"
        "s_addc_u32 s75, s9, 0\n"
        "global_load_lds_dwordx4 v8, s[74:75]\n"
        "s_add_u32 m0, m0, s62\n"
        "ds_read_b64_tr_b16 a[0:1], v12 offset:0\n"
        "global_load_lds_dwordx4 v9, s[74:75]\n"
        "s_add_u32 m0, m0, s62\n"
        "ds_read_b64_tr_b16 a[2:3], v12 offset:256\n"
        "global_load_lds_dwordx4 v10, s[74:75]\n"
        "s_add_u32 m0, m0, s62\n"
        "ds_read_b64_tr_b16 a[4:5], v12 offset:8192\n"
        "global_load_lds_dwordx4 v11, s[74:75]\n"
        "ds_read_b64_tr_b16 a[6:7], v12 offset:8448\n"
        "ds_read_b64_tr_b16 a[8:9], v13 offset:0\n"
        "ds_read_b64_tr_b16 a[10:11], v13 offset:256\n"
        "ds_read_b64_tr_b16 a[12:13], v13 offset:8192\n"
        "ds_read_b64_tr_b16 a[14:15], v13 offset:8448\n"
        "s_waitcnt vmcnt(29) lgkmcnt(0)\n"
        "s_barrier\n"
        "s_add_u32 m0, s32, 0\n"
        "s_add_u32 s74, s6, 2097152\n"
        "s_addc_u32 s75, s7, 0\n"
        "global_load_lds_dwordx4 v8, s[74:75]\n"
        "s_add_u32 m0, m0, s62\n"
        "ds_read_b64_tr_b16 a[128:129], v12 offset:16384\n"
        "global_load_lds_dwordx4 v9, s[74:75]\n"
        "s_add_u32 m0, m0, s62\n"
        "ds_read_b64_tr_b16 a[130:131], v12 offset:16640\n"
        "global_load_lds_dwordx4 v10, s[74:75]\n"
        "s_add_u32 m0, m0, s62\n"
        "ds_read_b64_tr_b16 a[132:133], v12 offset:24576\n"
        "global_load_lds_dwordx4 v11, s[74:75]\n"
        "ds_read_b64_tr_b16 a[134:135], v12 offset:24832\n"
        "ds_read_b64_tr_b16 a[136:137], v13 offset:16384\n"
        "ds_read_b64_tr_b16 a[138:139], v13 offset:16640\n"
        "ds_read_b64_tr_b16 a[140:141], v13 offset:24576\n"
        "ds_read_b64_tr_b16 a[142:143], v13 offset:24832\n"
        "s_waitcnt vmcnt(29) lgkmcnt(0)\n"
        "s_barrier\n"
        "s_add_u32 m0, s32, 16384\n"
        "s_add_u32 s74, s8, 2097152\n"
        "s_addc_u32 s75, s9, 0\n"
        "global_load_lds_dwordx4 v8, s[74:75]\n"
        "s_add_u32 m0, m0, s62\n"
        "ds_read_b64_tr_b16 a[16:17], v12 offset:32768\n"
        "global_load_lds_dwordx4 v9, s[74:75]\n"
        "s_add_u32 m0, m0, s62\n"
        "ds_read_b64_tr_b16 a[18:19], v12 offset:33024\n"
        "global_load_lds_dwordx4 v10, s[74:75]\n"
        "s_add_u32 m0, m0, s62\n"
        "ds_read_b64_tr_b16 a[20:21], v12 offset:40960\n"
        "global_load_lds_dwordx4 v11, s[74:75]\n"
        "ds_read_b64_tr_b16 a[22:23], v12 offset:41216\n"
        "ds_read_b64_tr_b16 a[24:25], v13 offset:32768\n"
        "ds_read_b64_tr_b16 a[26:27], v13 offset:33024\n"
        "ds_read_b64_tr_b16 a[28:29], v13 offset:40960\n"
        "ds_read_b64_tr_b16 a[30:31], v13 offset:41216\n"
        "s_waitcnt vmcnt(29) lgkmcnt(0)\n"
        "s_barrier\n"
        "s_add_u32 m0, s32, 32768\n"
        "s_add_u32 s74, s6, 2621440\n"
        "s_addc_u32 s75, s7, 0\n"
        "global_load_lds_dwordx4 v8, s[74:75]\n"
        "s_add_u32 m0, m0, s62\n"
        "ds_read_b64_tr_b16 a[144:145], v12 offset:49152\n"
        "global_load_lds_dwordx4 v9, s[74:75]\n"
        "s_add_u32 m0, m0, s62\n"
        "ds_read_b64_tr_b16 a[146:147], v12 offset:49408\n"
        "global_load_lds_dwordx4 v10, s[74:75]\n"
        "s_add_u32 m0, m0, s62\n"
        "ds_read_b64_tr_b16 a[148:149], v12 offset:57344\n"
        "global_load_lds_dwordx4 v11, s[74:75]\n"
        "ds_read_b64_tr_b16 a[150:151], v12 offset:57600\n"
        "ds_read_b64_tr_b16 a[152:153], v13 offset:49152\n"
        "ds_read_b64_tr_b16 a[154:155], v13 offset:49408\n"
        "ds_read_b64_tr_b16 a[156:157], v13 offset:57344\n"
        "ds_read_b64_tr_b16 a[158:159], v13 offset:57600\n"
        "s_waitcnt vmcnt(29) lgkmcnt(0)\n"
        "s_barrier\n"
        "s_add_u32 m0, s32, 49152\n"
        "s_add_u32 s74, s8, 2621440\n"
        "s_addc_u32 s75, s9, 0\n"
        "global_load_lds_dwordx4 v8, s[74:75]\n"
        "s_add_u32 m0, m0, s62\n"
        "ds_read_b64_tr_b16 a[32:33], v26 offset:0\n"
        "global_load_lds_dwordx4 v9, s[74:75]\n"
        "s_add_u32 m0, m0, s62\n"
        "ds_read_b64_tr_b16 a[34:35], v26 offset:256\n"
        "global_load_lds_dwordx4 v10, s[74:75]\n"
        "s_add_u32 m0, m0, s62\n"
        "ds_read_b64_tr_b16 a[36:37], v26 offset:8192\n"
        "global_load_lds_dwordx4 v11, s[74:75]\n"
        "ds_read_b64_tr_b16 a[38:39], v26 offset:8448\n"
        "ds_read_b64_tr_b16 a[40:41], v27 offset:0\n"
        "ds_read_b64_tr_b16 a[42:43], v27 offset:256\n"
        "ds_read_b64_tr_b16 a[44:45], v27 offset:8192\n"
        "ds_read_b64_tr_b16 a[46:47], v27 offset:8448\n"
        "s_waitcnt vmcnt(29) lgkmcnt(0)\n"
        "s_barrier\n"
        "s_add_u32 m0, s32, 65536\n"
        "s_add_u32 s74, s6, 3145728\n"
        "s_addc_u32 s75, s7, 0\n"
        "global_load_lds_dwordx4 v8, s[74:75]\n"
        "s_add_u32 m0, m0, s62\n"
        "ds_read_b64_tr_b16 a[160:161], v26 offset:16384\n"
        "global_load_lds_dwordx4 v9, s[74:75]\n"
        "s_add_u32 m0, m0, s62\n"
        "ds_read_b64_tr_b16 a[162:163], v26 offset:16640\n"
        "global_load_lds_dwordx4 v10, s[74:75]\n"
        "s_add_u32 m0, m0, s62\n"
        "ds_read_b64_tr_b16 a[164:165], v26 offset:24576\n"
        "global_load_lds_dwordx4 v11, s[74:75]\n"
        "ds_read_b64_tr_b16 a[166:167], v26 offset:24832\n"
        "ds_read_b64_tr_b16 a[168:169], v27 offset:16384\n"
        "ds_read_b64_tr_b16 a[170:171], v27 offset:16640\n"
        "ds_read_b64_tr_b16 a[172:173], v27 offset:24576\n"
        "ds_read_b64_tr_b16 a[174:175], v27 offset:24832\n"
        "s_waitcnt vmcnt(29) lgkmcnt(0)\n"
        "s_barrier\n"
        "s_add_u32 m0, s32, 81920\n"
        "s_add_u32 s74, s8, 3145728\n"
        "s_addc_u32 s75, s9, 0\n"
        "global_load_lds_dwordx4 v8, s[74:75]\n"
        "s_add_u32 m0, m0, s62\n"
        "ds_read_b64_tr_b16 a[48:49], v26 offset:32768\n"
        "global_load_lds_dwordx4 v9, s[74:75]\n"
        "s_add_u32 m0, m0, s62\n"
        "ds_read_b64_tr_b16 a[50:51], v26 offset:33024\n"
        "global_load_lds_dwordx4 v10, s[74:75]\n"
        "s_add_u32 m0, m0, s62\n"
        "ds_read_b64_tr_b16 a[52:53], v26 offset:40960\n"
        "global_load_lds_dwordx4 v11, s[74:75]\n"
        "ds_read_b64_tr_b16 a[54:55], v26 offset:41216\n"
        "ds_read_b64_tr_b16 a[56:57], v27 offset:32768\n"
        "ds_read_b64_tr_b16 a[58:59], v27 offset:33024\n"
        "ds_read_b64_tr_b16 a[60:61], v27 offset:40960\n"
        "ds_read_b64_tr_b16 a[62:63], v27 offset:41216\n"
        "s_waitcnt vmcnt(24) lgkmcnt(0)\n"
        "s_barrier\n"
        "s_add_u32 m0, s32, 98304\n"
        "s_add_u32 s74, s6, 3670016\n"
        "s_addc_u32 s75, s7, 0\n"
        "global_load_lds_dwordx4 v8, s[74:75]\n"
        "s_add_u32 m0, m0, s62\n"
        "ds_read_b64_tr_b16 a[176:177], v26 offset:49152\n"
        "global_load_lds_dwordx4 v9, s[74:75]\n"
        "s_add_u32 m0, m0, s62\n"
        "ds_read_b64_tr_b16 a[178:179], v26 offset:49408\n"
        "global_load_lds_dwordx4 v10, s[74:75]\n"
        "s_add_u32 m0, m0, s62\n"
        "ds_read_b64_tr_b16 a[180:181], v26 offset:57344\n"
        "global_load_lds_dwordx4 v11, s[74:75]\n"
        "ds_read_b64_tr_b16 a[182:183], v26 offset:57600\n"
        "ds_read_b64_tr_b16 a[184:185], v27 offset:49152\n"
        "ds_read_b64_tr_b16 a[186:187], v27 offset:49408\n"
        "ds_read_b64_tr_b16 a[188:189], v27 offset:57344\n"
        "ds_read_b64_tr_b16 a[190:191], v27 offset:57600\n"
        "s_mov_b32 s70, 0x05040100\n"
        "s_mov_b32 s71, 0x07060302\n"
        "s_lshl_b32 s64, s35, 11\n"
        "v_add_u32 v16, s64, v1\n"
        "v_perm_b32 v104, v98, v96, s70\n"
        "v_min_u32 v104, -2, v104\n"
        "v_perm_b32 v105, v98, v96, s71\n"
        "v_min_u32 v105, -2, v105\n"
        "v_perm_b32 v106, v99, v97, s70\n"
        "v_min_u32 v106, -2, v106\n"
        "v_perm_b32 v107, v99, v97, s71\n"
        "v_min_u32 v107, -2, v107\n"
        "v_perm_b32 v108, v102, v100, s70\n"
        "v_min_u32 v108, -2, v108\n"
        "v_perm_b32 v109, v102, v100, s71\n"
        "v_min_u32 v109, -2, v109\n"
        "v_perm_b32 v110, v103, v101, s70\n"
        "v_min_u32 v110, -2, v110\n"
        "v_perm_b32 v111, v103, v101, s71\n"
        "v_min_u32 v111, -2, v111\n"
        "ds_write_b128 v16, v[104:107] offset:0\n"
        "ds_write_b128 v16, v[108:111] offset:1024\n"
        "s_waitcnt vmcnt(24) lgkmcnt(0)\n"
        "s_barrier\n"
        "s_add_u32 m0, s32, 114688\n"
        "s_add_u32 s74, s8, 3670016\n"
        "s_addc_u32 s75, s9, 0\n"
        "global_load_lds_dwordx4 v8, s[74:75]\n"
        "s_add_u32 m0, m0, s62\n"
        "ds_read_b64_tr_b16 a[64:65], v12 offset:0\n"
        "global_load_lds_dwordx4 v9, s[74:75]\n"
        "s_add_u32 m0, m0, s62\n"
        "ds_read_b64_tr_b16 a[66:67], v12 offset:256\n"
        "global_load_lds_dwordx4 v10, s[74:75]\n"
        "s_add_u32 m0, m0, s62\n"
        "ds_read_b64_tr_b16 a[68:69], v12 offset:8192\n"
        "global_load_lds_dwordx4 v11, s[74:75]\n"
        "ds_read_b64_tr_b16 a[70:71], v12 offset:8448\n"
        "ds_read_b64_tr_b16 a[72:73], v13 offset:0\n"
        "ds_read_b64_tr_b16 a[74:75], v13 offset:256\n"
        "ds_read_b64_tr_b16 a[76:77], v13 offset:8192\n"
        "ds_read_b64_tr_b16 a[78:79], v13 offset:8448\n"
        "s_waitcnt vmcnt(24) lgkmcnt(0)\n"
        "s_barrier\n"
        "s_add_u32 m0, s32, 0\n"
        "s_add_u32 s74, s4, 2097152\n"
        "s_addc_u32 s75, s5, 0\n"
        "global_load_lds_dwordx4 v8, s[74:75]\n"
        "s_add_u32 m0, m0, s62\n"
        "ds_read_b64_tr_b16 a[192:193], v12 offset:16384\n"
        "global_load_lds_dwordx4 v9, s[74:75]\n"
        "s_add_u32 m0, m0, s62\n"
        "ds_read_b64_tr_b16 a[194:195], v12 offset:16640\n"
        "global_load_lds_dwordx4 v10, s[74:75]\n"
        "s_add_u32 m0, m0, s62\n"
        "ds_read_b64_tr_b16 a[196:197], v12 offset:24576\n"
        "global_load_lds_dwordx4 v11, s[74:75]\n"
        "ds_read_b64_tr_b16 a[198:199], v12 offset:24832\n"
        "ds_read_b64_tr_b16 a[200:201], v13 offset:16384\n"
        "ds_read_b64_tr_b16 a[202:203], v13 offset:16640\n"
        "ds_read_b64_tr_b16 a[204:205], v13 offset:24576\n"
        "ds_read_b64_tr_b16 a[206:207], v13 offset:24832\n"
        "s_waitcnt vmcnt(24) lgkmcnt(0)\n"
        "s_barrier\n"
        "s_add_u32 m0, s32, 16384\n"
        "s_add_u32 s74, s4, 2621440\n"
        "s_addc_u32 s75, s5, 0\n"
        "global_load_lds_dwordx4 v8, s[74:75]\n"
        "s_add_u32 m0, m0, s62\n"
        "ds_read_b64_tr_b16 a[80:81], v12 offset:32768\n"
        "global_load_lds_dwordx4 v9, s[74:75]\n"
        "s_add_u32 m0, m0, s62\n"
        "ds_read_b64_tr_b16 a[82:83], v12 offset:33024\n"
        "global_load_lds_dwordx4 v10, s[74:75]\n"
        "s_add_u32 m0, m0, s62\n"
        "ds_read_b64_tr_b16 a[84:85], v12 offset:40960\n"
        "global_load_lds_dwordx4 v11, s[74:75]\n"
        "ds_read_b64_tr_b16 a[86:87], v12 offset:41216\n"
        "ds_read_b64_tr_b16 a[88:89], v13 offset:32768\n"
        "ds_read_b64_tr_b16 a[90:91], v13 offset:33024\n"
        "ds_read_b64_tr_b16 a[92:93], v13 offset:40960\n"
        "ds_read_b64_tr_b16 a[94:95], v13 offset:41216\n"
        "s_waitcnt vmcnt(24) lgkmcnt(0)\n"
        "s_barrier\n"
        "s_add_u32 m0, s32, 32768\n"
        "s_add_u32 s74, s4, 3145728\n"
        "s_addc_u32 s75, s5, 0\n"
        "global_load_lds_dwordx4 v8, s[74:75]\n"
        "s_add_u32 m0, m0, s62\n"
        "ds_read_b64_tr_b16 a[208:209], v12 offset:49152\n"
        "global_load_lds_dwordx4 v9, s[74:75]\n"
        "s_add_u32 m0, m0, s62\n"
        "ds_read_b64_tr_b16 a[210:211], v12 offset:49408\n"
        "global_load_lds_dwordx4 v10, s[74:75]\n"
        "s_add_u32 m0, m0, s62\n"
        "ds_read_b64_tr_b16 a[212:213], v12 offset:57344\n"
        "global_load_lds_dwordx4 v11, s[74:75]\n"
        "ds_read_b64_tr_b16 a[214:215], v12 offset:57600\n"
        "ds_read_b64_tr_b16 a[216:217], v13 offset:49152\n"
        "ds_read_b64_tr_b16 a[218:219], v13 offset:49408\n"
        "ds_read_b64_tr_b16 a[220:221], v13 offset:57344\n"
        "ds_read_b64_tr_b16 a[222:223], v13 offset:57600\n"
        "s_waitcnt vmcnt(24) lgkmcnt(0)\n"
        "s_barrier\n"
        "s_add_u32 m0, s32, 49152\n"
        "s_add_u32 s74, s4, 3670016\n"
        "s_addc_u32 s75, s5, 0\n"
        "global_load_lds_dwordx4 v8, s[74:75]\n"
        "s_add_u32 m0, m0, s62\n"
        "ds_read_b64_tr_b16 a[96:97], v26 offset:0\n"
        "global_load_lds_dwordx4 v9, s[74:75]\n"
        "s_add_u32 m0, m0, s62\n"
        "ds_read_b64_tr_b16 a[98:99], v26 offset:256\n"
        "global_load_lds_dwordx4 v10, s[74:75]\n"
        "s_add_u32 m0, m0, s62\n"
        "ds_read_b64_tr_b16 a[100:101], v26 offset:8192\n"
        "global_load_lds_dwordx4 v11, s[74:75]\n"
        "ds_read_b64_tr_b16 a[102:103], v26 offset:8448\n"
        "ds_read_b64_tr_b16 a[104:105], v27 offset:0\n"
        "ds_read_b64_tr_b16 a[106:107], v27 offset:256\n"
        "ds_read_b64_tr_b16 a[108:109], v27 offset:8192\n"
        "ds_read_b64_tr_b16 a[110:111], v27 offset:8448\n"
        "s_waitcnt vmcnt(24) lgkmcnt(0)\n"
        "s_barrier\n"
        "s_add_u32 m0, s32, 65536\n"
        "s_add_u32 s74, s10, 0\n"
        "s_addc_u32 s75, s11, 0\n"
        "global_load_lds_dwordx4 v20, s[74:75]\n"
        "s_add_u32 m0, m0, s62\n"
        "ds_read_b64_tr_b16 a[224:225], v26 offset:16384\n"
        "global_load_lds_dwordx4 v21, s[74:75]\n"
        "s_add_u32 m0, m0, s62\n"
        "ds_read_b64_tr_b16 a[226:227], v26 offset:16640\n"
        "global_load_lds_dwordx4 v22, s[74:75]\n"
        "s_add_u32 m0, m0, s62\n"
        "ds_read_b64_tr_b16 a[228:229], v26 offset:24576\n"
        "global_load_lds_dwordx4 v23, s[74:75]\n"
        "ds_read_b64_tr_b16 a[230:231], v26 offset:24832\n"
        "ds_read_b64_tr_b16 a[232:233], v27 offset:16384\n"
        "ds_read_b64_tr_b16 a[234:235], v27 offset:16640\n"
        "ds_read_b64_tr_b16 a[236:237], v27 offset:24576\n"
        "ds_read_b64_tr_b16 a[238:239], v27 offset:24832\n"
        "s_waitcnt vmcnt(24) lgkmcnt(0)\n"
        "s_barrier\n"
        "s_add_u32 m0, s32, 81920\n"
        "s_add_u32 s74, s10, 65536\n"
        "s_addc_u32 s75, s11, 0\n"
        "global_load_lds_dwordx4 v20, s[74:75]\n"
        "s_add_u32 m0, m0, s62\n"
        "ds_read_b64_tr_b16 a[112:113], v26 offset:32768\n"
        "global_load_lds_dwordx4 v21, s[74:75]\n"
        "s_add_u32 m0, m0, s62\n"
        "ds_read_b64_tr_b16 a[114:115], v26 offset:33024\n"
        "global_load_lds_dwordx4 v22, s[74:75]\n"
        "s_add_u32 m0, m0, s62\n"
        "ds_read_b64_tr_b16 a[116:117], v26 offset:40960\n"
        "global_load_lds_dwordx4 v23, s[74:75]\n"
        "ds_read_b64_tr_b16 a[118:119], v26 offset:41216\n"
        "ds_read_b64_tr_b16 a[120:121], v27 offset:32768\n"
        "ds_read_b64_tr_b16 a[122:123], v27 offset:33024\n"
        "ds_read_b64_tr_b16 a[124:125], v27 offset:40960\n"
        "ds_read_b64_tr_b16 a[126:127], v27 offset:41216\n"
        "global_load_dword v25, v18, s[20:21] sc1\n"
        "s_waitcnt vmcnt(25) lgkmcnt(0)\n"
        "s_barrier\n"
        "s_add_u32 m0, s32, 98304\n"
        "s_add_u32 s74, s10, 256\n"
        "s_addc_u32 s75, s11, 0\n"
        "global_load_lds_dwordx4 v20, s[74:75]\n"
        "s_add_u32 m0, m0, s62\n"
        "ds_read_b64_tr_b16 a[240:241], v26 offset:49152\n"
        "global_load_lds_dwordx4 v21, s[74:75]\n"
        "s_add_u32 m0, m0, s62\n"
        "ds_read_b64_tr_b16 a[242:243], v26 offset:49408\n"
        "global_load_lds_dwordx4 v22, s[74:75]\n"
        "s_add_u32 m0, m0, s62\n"
        "ds_read_b64_tr_b16 a[244:245], v26 offset:57344\n"
        "global_load_lds_dwordx4 v23, s[74:75]\n"
        "ds_read_b64_tr_b16 a[246:247], v26 offset:57600\n"
        "ds_read_b64_tr_b16 a[248:249], v27 offset:49152\n"
        "ds_read_b64_tr_b16 a[250:251], v27 offset:49408\n"
        "ds_read_b64_tr_b16 a[252:253], v27 offset:57344\n"
        "ds_read_b64_tr_b16 a[254:255], v27 offset:57600\n"
        "s_waitcnt vmcnt(25) lgkmcnt(0)\n"
        "s_barrier\n"
        "s_add_u32 m0, s32, 114688\n"
        "s_add_u32 s74, s10, 65792\n"
        "s_addc_u32 s75, s11, 0\n"
        "global_load_lds_dwordx4 v20, s[74:75]\n"
        "s_add_u32 m0, m0, s62\n"
        "ds_read_b64_tr_b16 v[128:129], v12 offset:0\n"
        "global_load_lds_dwordx4 v21, s[74:75]\n"
        "s_add_u32 m0, m0, s62\n"
        "ds_read_b64_tr_b16 v[130:131], v12 offset:256\n"
        "global_load_lds_dwordx4 v22, s[74:75]\n"
        "s_add_u32 m0, m0, s62\n"
        "ds_read_b64_tr_b16 v[132:133], v12 offset:8192\n"
        "global_load_lds_dwordx4 v23, s[74:75]\n"
        "ds_read_b64_tr_b16 v[134:135], v12 offset:8448\n"
        "ds_read_b64_tr_b16 v[136:137], v13 offset:0\n"
        "ds_read_b64_tr_b16 v[138:139], v13 offset:256\n"
        "ds_read_b64_tr_b16 v[140:141], v13 offset:8192\n"
        "ds_read_b64_tr_b16 v[142:143], v13 offset:8448\n"
        "s_waitcnt vmcnt(25) lgkmcnt(0)\n"
        "s_barrier\n"
        "s_add_u32 m0, s32, 0\n"
        "s_add_u32 s74, s4, 0\n"
        "s_addc_u32 s75, s5, 0\n"
        "global_load_lds_dwordx4 v8, s[74:75]\n"
        "s_add_u32 m0, m0, s62\n"
        "ds_read_b64_tr_b16 v[144:145], v12 offset:16384\n"
        "global_load_lds_dwordx4 v9, s[74:75]\n"
        "s_add_u32 m0, m0, s62\n"
        "ds_read_b64_tr_b16 v[146:147], v12 offset:16640\n"
        "global_load_lds_dwordx4 v10, s[74:75]\n"
        "s_add_u32 m0, m0, s62\n"
        "ds_read_b64_tr_b16 v[148:149], v12 offset:24576\n"
        "global_load_lds_dwordx4 v11, s[74:75]\n"
        "ds_read_b64_tr_b16 v[150:151], v12 offset:24832\n"
        "ds_read_b64_tr_b16 v[152:153], v13 offset:16384\n"
        "ds_read_b64_tr_b16 v[154:155], v13 offset:16640\n"
        "ds_read_b64_tr_b16 v[156:157], v13 offset:24576\n"
        "ds_read_b64_tr_b16 v[158:159], v13 offset:24832\n"
        "s_waitcnt vmcnt(25) lgkmcnt(0)\n"
        "s_barrier\n"
        "s_add_u32 m0, s32, 16384\n"
        "s_add_u32 s74, s4, 524288\n"
        "s_addc_u32 s75, s5, 0\n"
        "global_load_lds_dwordx4 v8, s[74:75]\n"
        "s_add_u32 m0, m0, s62\n"
        "ds_read_b64_tr_b16 v[160:161], v12 offset:32768\n"
        "global_load_lds_dwordx4 v9, s[74:75]\n"
        "s_add_u32 m0, m0, s62\n"
        "ds_read_b64_tr_b16 v[162:163], v12 offset:33024\n"
        "global_load_lds_dwordx4 v10, s[74:75]\n"
        "s_add_u32 m0, m0, s62\n"
        "ds_read_b64_tr_b16 v[164:165], v12 offset:40960\n"
        "global_load_lds_dwordx4 v11, s[74:75]\n"
        "ds_read_b64_tr_b16 v[166:167], v12 offset:41216\n"
        "ds_read_b64_tr_b16 v[168:169], v13 offset:32768\n"
        "ds_read_b64_tr_b16 v[170:171], v13 offset:33024\n"
        "ds_read_b64_tr_b16 v[172:173], v13 offset:40960\n"
        "ds_read_b64_tr_b16 v[174:175], v13 offset:41216\n"
        "s_waitcnt vmcnt(25) lgkmcnt(0)\n"
        "s_barrier\n"
        "s_add_u32 m0, s32, 32768\n"
        "s_add_u32 s74, s4, 1048576\n"
        "s_addc_u32 s75, s5, 0\n"
        "global_load_lds_dwordx4 v8, s[74:75]\n"
        "s_add_u32 m0, m0, s62\n"
        "ds_read_b64_tr_b16 v[176:177], v12 offset:49152\n"
        "global_load_lds_dwordx4 v9, s[74:75]\n"
        "s_add_u32 m0, m0, s62\n"
        "ds_read_b64_tr_b16 v[178:179], v12 offset:49408\n"
        "global_load_lds_dwordx4 v10, s[74:75]\n"
        "s_add_u32 m0, m0, s62\n"
        "ds_read_b64_tr_b16 v[180:181], v12 offset:57344\n"
        "global_load_lds_dwordx4 v11, s[74:75]\n"
        "ds_read_b64_tr_b16 v[182:183], v12 offset:57600\n"
        "ds_read_b64_tr_b16 v[184:185], v13 offset:49152\n"
        "ds_read_b64_tr_b16 v[186:187], v13 offset:49408\n"
        "ds_read_b64_tr_b16 v[188:189], v13 offset:57344\n"
        "ds_read_b64_tr_b16 v[190:191], v13 offset:57600\n"
        "s_waitcnt vmcnt(25) lgkmcnt(0)\n"
        "s_barrier\n"
        "s_add_u32 m0, s32, 49152\n"
        "s_add_u32 s74, s4, 1572864\n"
        "s_addc_u32 s75, s5, 0\n"
        "global_load_lds_dwordx4 v8, s[74:75]\n"
        "s_add_u32 m0, m0, s62\n"
        "ds_read_b64_tr_b16 v[192:193], v26 offset:0\n"
        "global_load_lds_dwordx4 v9, s[74:75]\n"
        "s_add_u32 m0, m0, s62\n"
        "ds_read_b64_tr_b16 v[194:195], v26 offset:256\n"
        "global_load_lds_dwordx4 v10, s[74:75]\n"
        "s_add_u32 m0, m0, s62\n"
        "ds_read_b64_tr_b16 v[196:197], v26 offset:8192\n"
        "global_load_lds_dwordx4 v11, s[74:75]\n"
        "ds_read_b64_tr_b16 v[198:199], v26 offset:8448\n"
        "ds_read_b64_tr_b16 v[200:201], v27 offset:0\n"
        "ds_read_b64_tr_b16 v[202:203], v27 offset:256\n"
        "ds_read_b64_tr_b16 v[204:205], v27 offset:8192\n"
        "ds_read_b64_tr_b16 v[206:207], v27 offset:8448\n"
        "s_waitcnt vmcnt(25) lgkmcnt(0)\n"
        "s_barrier\n"
        "s_add_u32 m0, s32, 65536\n"
        "s_add_u32 s74, s10, 512\n"
        "s_addc_u32 s75, s11, 0\n"
        "global_load_lds_dwordx4 v20, s[74:75]\n"
        "s_add_u32 m0, m0, s62\n"
        "ds_read_b64_tr_b16 v[208:209], v26 offset:16384\n"
        "global_load_lds_dwordx4 v21, s[74:75]\n"
        "s_add_u32 m0, m0, s62\n"
        "ds_read_b64_tr_b16 v[210:211], v26 offset:16640\n"
        "global_load_lds_dwordx4 v22, s[74:75]\n"
        "s_add_u32 m0, m0, s62\n"
        "ds_read_b64_tr_b16 v[212:213], v26 offset:24576\n"
        "global_load_lds_dwordx4 v23, s[74:75]\n"
        "ds_read_b64_tr_b16 v[214:215], v26 offset:24832\n"
        "ds_read_b64_tr_b16 v[216:217], v27 offset:16384\n"
        "ds_read_b64_tr_b16 v[218:219], v27 offset:16640\n"
        "ds_read_b64_tr_b16 v[220:221], v27 offset:24576\n"
        "ds_read_b64_tr_b16 v[222:223], v27 offset:24832\n"
        "s_waitcnt vmcnt(24) lgkmcnt(0)\n"
        "s_barrier\n"
        "s_add_u32 m0, s32, 81920\n"
        "s_add_u32 s74, s10, 66048\n"
        "s_addc_u32 s75, s11, 0\n"
        "global_load_lds_dwordx4 v20, s[74:75]\n"
        "s_add_u32 m0, m0, s62\n"
        "ds_read_b64_tr_b16 v[224:225], v26 offset:32768\n"
        "global_load_lds_dwordx4 v21, s[74:75]\n"
        "s_add_u32 m0, m0, s62\n"
        "ds_read_b64_tr_b16 v[226:227], v26 offset:33024\n"
        "global_load_lds_dwordx4 v22, s[74:75]\n"
        "s_add_u32 m0, m0, s62\n"
        "ds_read_b64_tr_b16 v[228:229], v26 offset:40960\n"
        "global_load_lds_dwordx4 v23, s[74:75]\n"
        "ds_read_b64_tr_b16 v[230:231], v26 offset:41216\n"
        "ds_read_b64_tr_b16 v[232:233], v27 offset:32768\n"
        "ds_read_b64_tr_b16 v[234:235], v27 offset:33024\n"
        "ds_read_b64_tr_b16 v[236:237], v27 offset:40960\n"
        "ds_read_b64_tr_b16 v[238:239], v27 offset:41216\n"
        "s_waitcnt vmcnt(24) lgkmcnt(0)\n"
        "s_barrier\n"
        "s_add_u32 m0, s32, 98304\n"
        "s_add_u32 s74, s10, 768\n"
        "s_addc_u32 s75, s11, 0\n"
        "global_load_lds_dwordx4 v20, s[74:75]\n"
        "s_add_u32 m0, m0, s62\n"
        "ds_read_b64_tr_b16 v[240:241], v26 offset:49152\n"
        "global_load_lds_dwordx4 v21, s[74:75]\n"
        "s_add_u32 m0, m0, s62\n"
        "ds_read_b64_tr_b16 v[242:243], v26 offset:49408\n"
        "global_load_lds_dwordx4 v22, s[74:75]\n"
        "s_add_u32 m0, m0, s62\n"
        "ds_read_b64_tr_b16 v[244:245], v26 offset:57344\n"
        "global_load_lds_dwordx4 v23, s[74:75]\n"
        "ds_read_b64_tr_b16 v[246:247], v26 offset:57600\n"
        "ds_read_b64_tr_b16 v[248:249], v27 offset:49152\n"
        "ds_read_b64_tr_b16 v[250:251], v27 offset:49408\n"
        "ds_read_b64_tr_b16 v[252:253], v27 offset:57344\n"
        "ds_read_b64_tr_b16 v[254:255], v27 offset:57600\n"
        "s_waitcnt vmcnt(24) lgkmcnt(0)\n"
        "s_barrier\n"
        "s_add_u32 m0, s32, 114688\n"
        "s_add_u32 s74, s10, 66304\n"
        "s_addc_u32 s75, s11, 0\n"
        "global_load_lds_dwordx4 v20, s[74:75]\n"
        "s_add_u32 m0, m0, s62\n"
        "ds_read_b64_tr_b16 v[64:65], v12 offset:0\n"
        "global_load_lds_dwordx4 v21, s[74:75]\n"
        "s_add_u32 m0, m0, s62\n"
        "ds_read_b64_tr_b16 v[66:67], v12 offset:256\n"
        "global_load_lds_dwordx4 v22, s[74:75]\n"
        "s_add_u32 m0, m0, s62\n"
        "ds_read_b64_tr_b16 v[68:69], v12 offset:8192\n"
        "global_load_lds_dwordx4 v23, s[74:75]\n"
        "ds_read_b64_tr_b16 v[70:71], v12 offset:8448\n"
        "ds_read_b64_tr_b16 v[72:73], v13 offset:0\n"
        "ds_read_b64_tr_b16 v[74:75], v13 offset:256\n"
        "ds_read_b64_tr_b16 v[76:77], v13 offset:8192\n"
        "ds_read_b64_tr_b16 v[78:79], v13 offset:8448\n"
        "s_waitcnt vmcnt(24) lgkmcnt(0)\n"
        "s_barrier\n"
        "ds_write_b128 v6, v[64:67] offset:0\n"
        "ds_write_b128 v6, v[68:71] offset:1024\n"
        "ds_write_b128 v6, v[72:75] offset:2048\n"
        "ds_write_b128 v6, v[76:79] offset:3072\n"
        "ds_read_b64_tr_b16 v[80:81], v12 offset:16384\n"
        "ds_read_b64_tr_b16 v[82:83], v12 offset:16640\n"
        "ds_read_b64_tr_b16 v[84:85], v12 offset:24576\n"
        "ds_read_b64_tr_b16 v[86:87], v12 offset:24832\n"
        "ds_read_b64_tr_b16 v[88:89], v13 offset:16384\n"
        "ds_read_b64_tr_b16 v[90:91], v13 offset:16640\n"
        "ds_read_b64_tr_b16 v[92:93], v13 offset:24576\n"
        "ds_read_b64_tr_b16 v[94:95], v13 offset:24832\n"
        "s_waitcnt vmcnt(20) lgkmcnt(0)\n"
        "s_barrier\n"
        "ds_write_b128 v6, v[80:83] offset:16384\n"
        "ds_write_b128 v6, v[84:87] offset:17408\n"
        "ds_write_b128 v6, v[88:91] offset:18432\n"
        "ds_write_b128 v6, v[92:95] offset:19456\n"
        "ds_read_b64_tr_b16 v[64:65], v12 offset:32768\n"
        "ds_read_b64_tr_b16 v[66:67], v12 offset:33024\n"
        "ds_read_b64_tr_b16 v[68:69], v12 offset:40960\n"
        "ds_read_b64_tr_b16 v[70:71], v12 offset:41216\n"
        "ds_read_b64_tr_b16 v[72:73], v13 offset:32768\n"
        "ds_read_b64_tr_b16 v[74:75], v13 offset:33024\n"
        "ds_read_b64_tr_b16 v[76:77], v13 offset:40960\n"
        "ds_read_b64_tr_b16 v[78:79], v13 offset:41216\n"
        "s_waitcnt vmcnt(16) lgkmcnt(0)\n"
        "s_barrier\n"
        "ds_write_b128 v6, v[64:67] offset:32768\n"
        "ds_write_b128 v6, v[68:71] offset:33792\n"
        "ds_write_b128 v6, v[72:75] offset:34816\n"
        "ds_write_b128 v6, v[76:79] offset:35840\n"
        "ds_read_b64_tr_b16 v[80:81], v12 offset:49152\n"
        "ds_read_b64_tr_b16 v[82:83], v12 offset:49408\n"
        "ds_read_b64_tr_b16 v[84:85], v12 offset:57344\n"
        "ds_read_b64_tr_b16 v[86:87], v12 offset:57600\n"
        "ds_read_b64_tr_b16 v[88:89], v13 offset:49152\n"
        "ds_read_b64_tr_b16 v[90:91], v13 offset:49408\n"
        "ds_read_b64_tr_b16 v[92:93], v13 offset:57344\n"
        "ds_read_b64_tr_b16 v[94:95], v13 offset:57600\n"
        "s_waitcnt vmcnt(12) lgkmcnt(0)\n"
        "s_barrier\n"
        "ds_write_b128 v6, v[80:83] offset:49152\n"
        "ds_write_b128 v6, v[84:87] offset:50176\n"
        "ds_write_b128 v6, v[88:91] offset:51200\n"
        "ds_write_b128 v6, v[92:95] offset:52224\n"
        "ds_read_b64_tr_b16 v[64:65], v26 offset:0\n"
        "ds_read_b64_tr_b16 v[66:67], v26 offset:256\n"
        "ds_read_b64_tr_b16 v[68:69], v26 offset:8192\n"
        "ds_read_b64_tr_b16 v[70:71], v26 offset:8448\n"
        "ds_read_b64_tr_b16 v[72:73], v27 offset:0\n"
        "ds_read_b64_tr_b16 v[74:75], v27 offset:256\n"
        "ds_read_b64_tr_b16 v[76:77], v27 offset:8192\n"
        "ds_read_b64_tr_b16 v[78:79], v27 offset:8448\n"
        "s_waitcnt vmcnt(8) lgkmcnt(0)\n"
        "s_barrier\n"
        "ds_write_b128 v28, v[64:67] offset:0\n"
        "ds_write_b128 v28, v[68:71] offset:1024\n"
        "ds_write_b128 v28, v[72:75] offset:2048\n"
        "ds_write_b128 v28, v[76:79] offset:3072\n"
        "ds_read_b64_tr_b16 v[80:81], v26 offset:16384\n"
        "ds_read_b64_tr_b16 v[82:83], v26 offset:16640\n"
        "ds_read_b64_tr_b16 v[84:85], v26 offset:24576\n"
        "ds_read_b64_tr_b16 v[86:87], v26 offset:24832\n"
        "ds_read_b64_tr_b16 v[88:89], v27 offset:16384\n"
        "ds_read_b64_tr_b16 v[90:91], v27 offset:16640\n"
        "ds_read_b64_tr_b16 v[92:93], v27 offset:24576\n"
        "ds_read_b64_tr_b16 v[94:95], v27 offset:24832\n"
        "s_waitcnt vmcnt(4) lgkmcnt(0)\n"
        "s_barrier\n"
        "ds_write_b128 v28, v[80:83] offset:16384\n"
        "ds_write_b128 v28, v[84:87] offset:17408\n"
        "ds_write_b128 v28, v[88:91] offset:18432\n"
        "ds_write_b128 v28, v[92:95] offset:19456\n"
        "ds_read_b64_tr_b16 v[64:65], v26 offset:32768\n"
        "ds_read_b64_tr_b16 v[66:67], v26 offset:33024\n"
        "ds_read_b64_tr_b16 v[68:69], v26 offset:40960\n"
        "ds_read_b64_tr_b16 v[70:71], v26 offset:41216\n"
        "ds_read_b64_tr_b16 v[72:73], v27 offset:32768\n"
        "ds_read_b64_tr_b16 v[74:75], v27 offset:33024\n"
        "ds_read_b64_tr_b16 v[76:77], v27 offset:40960\n"
        "ds_read_b64_tr_b16 v[78:79], v27 offset:41216\n"
        "s_waitcnt vmcnt(0) lgkmcnt(0)\n"
        "s_barrier\n"
        "ds_write_b128 v28, v[64:67] offset:32768\n"
        "ds_write_b128 v28, v[68:71] offset:33792\n"
        "ds_write_b128 v28, v[72:75] offset:34816\n"
        "ds_write_b128 v28, v[76:79] offset:35840\n"
        "ds_read_b64_tr_b16 v[80:81], v26 offset:49152\n"
        "ds_read_b64_tr_b16 v[82:83], v26 offset:49408\n"
        "ds_read_b64_tr_b16 v[84:85], v26 offset:57344\n"
        "ds_read_b64_tr_b16 v[86:87], v26 offset:57600\n"
        "ds_read_b64_tr_b16 v[88:89], v27 offset:49152\n"
        "ds_read_b64_tr_b16 v[90:91], v27 offset:49408\n"
        "ds_read_b64_tr_b16 v[92:93], v27 offset:57344\n"
        "ds_read_b64_tr_b16 v[94:95], v27 offset:57600\n"
        "s_waitcnt lgkmcnt(0)\n"
        "s_barrier\n"
        "ds_write_b128 v28, v[80:83] offset:49152\n"
        "ds_write_b128 v28, v[84:87] offset:50176\n"
        "ds_write_b128 v28, v[88:91] offset:51200\n"
        "ds_write_b128 v28, v[92:95] offset:52224\n"
        "s_cmp_eq_u32 s35, 0\n"
        "s_cbranch_scc0 Lw3%=\n"
        "v_readfirstlane_b32 s72, v24\n"
        "v_mov_b32 v19, 1\n"
        "v_mov_b32 v23, s73\n"
        "v_mov_b32 v26, s93\n"
        "v_writelane_b32 v26, s94, 1\n"
        "Lwt%=:\n"
        "v_readfirstlane_b32 s65, v25\n"
        "s_cmp_eq_u32 s65, s72\n"
        "s_cbranch_scc0 Lw0%=\n"
        "s_add_u32 s67, s67, 1\n"
        "s_cmp_gt_u32 s67, 262144\n"
        "s_cbranch_scc1 Lfail%=\n"
        "global_load_dword v25, v18, s[56:57] sc1\n"
        "s_waitcnt vmcnt(0)\n"
        "s_branch Lwt%=\n"
        "Lw0%=:\n"
        "s_add_u32 s67, s67, 1\n"
        "s_cmp_gt_u32 s67, 262144\n"
        "s_cbranch_scc1 Lfail%=\n"
        "global_load_dwordx2 v[20:21], v18, s[56:57] offset:2048 sc1\n"
        "global_load_dword v22, v26, s[56:57] sc1\n"
        "s_waitcnt vmcnt(0)\n"
        "v_readfirstlane_b32 s66, v21\n"
        "v_cmp_gt_u32 vcc, 2, v22\n"
        "s_xor_b32 s66, s66, 32\n"
        "s_and_b32 s65, vcc_lo, 0xff\n"
        "s_or_b32 s65, s65, s66\n"
        "s_cbranch_scc0 Lw1%=\n"
        "s_sleep 1\n"
        "s_branch Lw0%=\n"
        "Lw1%=:\n"
        "ds_write_b32 v18, v20 offset:25088\n"
        "Lw3%=:\n"
        "s_waitcnt lgkmcnt(0)\n"
        "s_barrier\n"
        "s_mov_b32 m0, s37\n"
        "s_nop 0\n"
        "global_load_lds_dwordx4 v1, s[38:39] sc1\n"
        "global_load_lds_dwordx4 v1, s[38:39] offset:1024 sc1\n"
        "s_mov_b32 m0, s55\n"
        "s_nop 0\n"
        "global_load_lds_dwordx4 v1, s[40:41] sc1\n"
        "global_load_lds_dwordx4 v1, s[40:41] offset:1024 sc1\n"
        "ds_read_b32 v20, v18 offset:25088\n"
        "ds_read_b128 v[64:67], v6 offset:0\n"
        "ds_read_b128 v[68:71], v6 offset:1024\n"
        "ds_read_b128 v[72:75], v6 offset:2048\n"
        "ds_read_b128 v[76:79], v6 offset:3072\n"
        "ds_read_b128 v[80:83], v6 offset:16384\n"
        "ds_read_b128 v[84:87], v6 offset:17408\n"
        "ds_read_b128 v[88:91], v6 offset:18432\n"
        "ds_read_b128 v[92:95], v6 offset:19456\n"
        "ds_read_b128 v[96:99], v6 offset:32768\n"
        "ds_read_b128 v[100:103], v6 offset:33792\n"
        "ds_read_b128 v[104:107], v6 offset:34816\n"
        "ds_read_b128 v[108:111], v6 offset:35840\n"
        "ds_read_b128 v[112:115], v6 offset:49152\n"
        "ds_read_b128 v[116:119], v6 offset:50176\n"
        "s_waitcnt lgkmcnt(14)\n"
        "v_readfirstlane_b32 s64, v20\n"
        "ds_read_b128 v[120:123], v6 offset:51200\n"
        "ds_read_b128 v[124:127], v6 offset:52224\n"
        "s_and_b32 s66, s64, 0xff\n"
        "s_mov_b32 s65, 0x4000\n"
        "s_bfe_u32 s70, s64, 0x80008\n"
        "s_cmp_gt_u32 s70, s66\n"
        "s_cselect_b32 s66, s70, s66\n"
        "s_cselect_b32 s65, 0x6000, s65\n"
        "s_bfe_u32 s70, s64, 0x80010\n"
        "s_cmp_gt_u32 s70, s66\n"
        "s_cselect_b32 s66, s70, s66\n"
        "s_cselect_b32 s65, 0x0, s65\n"
        "s_bfe_u32 s70, s64, 0x80018\n"
        "s_cmp_gt_u32 s70, s66\n"
        "s_cselect_b32 s66, s70, s66\n"
        "s_cselect_b32 s65, 0x2000, s65\n"
        "s_xor_b32 s44, s44, s65\n"
        "s_xor_b32 s46, s46, s65\n"
        "s_xor_b32 s48, s48, s65\n"
        "s_xor_b32 s50, s50, s65\n"
        "s_xor_b32 s52, s52, s65\n"
        "s_xor_b32 s88, s88, s65\n"
        "s_xor_b32 s78, s78, s65\n"
        "s_sub_u32 s88, s88, s56\n"
        "s_mov_b32 s32, 90\n"
        "s_mov_b32 s72, 250\n"
        "s_add_u32 s38, s38, 0x40000\n"
        "s_addc_u32 s39, s39, 0\n"
        "s_add_u32 s40, s40, 0x40000\n"
        "s_addc_u32 s41, s41, 0\n"
        "v_mov_b32 v34, v19\n"
        "v_mov_b32 v35, v23\n"
        "v_mov_b32 v8, 0\n"
        "v_mov_b32 v9, 0\n"
        "v_mov_b32 v10, 0\n"
        "v_mov_b32 v11, 0\n"
        "v_mov_b32 v12, 0\n"
        "v_mov_b32 v13, 0\n"
        "v_mov_b32 v14, 0\n"
        "v_mov_b32 v15, 0\n"
        "v_mov_b32 v16, 0\n"
        "v_mov_b32 v17, 0\n"
        "v_mov_b32 v18, 0\n"
        "v_mov_b32 v19, 0\n"
        "v_mov_b32 v20, 0\n"
        "v_mov_b32 v21, 0\n"
        "v_mov_b32 v22, 0\n"
        "v_mov_b32 v23, 0\n"
        "v_mov_b32 v24, 0\n"
        "v_mov_b32 v25, 0\n"
        "v_mov_b32 v26, 0\n"
        "v_mov_b32 v27, 0\n"
        "v_mov_b32 v28, 0\n"
        "v_mov_b32 v29, 0\n"
        "v_mov_b32 v30, 0\n"
        "v_mov_b32 v31, 0\n"
        "s_waitcnt vmcnt(0) lgkmcnt(0)\n"
        "s_barrier\n"
        "s_cmp_eq_u32 s35, 1\n"
        "s_cbranch_scc1 Lw1step%=\n"
        "s_cmp_eq_u32 s35, 2\n"
        "s_cbranch_scc1 Lw2step%=\n"
        "s_cmp_eq_u32 s35, 3\n"
        "s_cbranch_scc1 Lw3step%=\n"
        "s_memrealtime s[0:1]\n"
        "s_waitcnt lgkmcnt(0)\n"
        "s_lshl_b32 s0, s0, 3\n"
        "s_add_u32 s62, s0, s61\n"
        "s_add_u32 s65, s62, s63\n"
        "s_lshl_b32 s65, s65, 8\n"
        "s_add_u32 s65, s65, s36\n"
        "v_add_u32 v33, s65, 1\n"
        "s_mov_b64 exec, 1\n"
        "global_store_dword v1, v33, s[78:79] sc1\n"
        "s_mov_b64 exec, -1\n"
        "s_mov_b64 exec, 1\n"
        "global_atomic_add v35, v34, s[56:57]\n"
        "s_mov_b64 exec, -1\n"
        "s_branch Lw0in%=\n"
        "Lw0step%=:\n"
        "s_waitcnt vmcnt(0)\n"
        "Lw0in%=:\n"
        "s_barrier\n"
        "ds_read_b128 v[32:35], v1 offset:0\n"
        "ds_read_b128 v[36:39], v1 offset:1024\n"
        "s_waitcnt lgkmcnt(1)\n"
        "v_smfmac_f32_16x16x64_bf16 v[8:11], v[32:35], v[64:71], v0\n"
        "v_smfmac_f32_16x16x64_bf16 v[12:15], v[32:35], v[72:79], v0\n"
        "s_mov_b64 exec, 1\n"
        "global_store_dword v1, v59, s[76:77] sc1\n"
        "s_mov_b64 exec, -1\n"
        "s_waitcnt lgkmcnt(0)\n"
        "v_smfmac_f32_16x16x64_bf16 v[8:11], v[36:39], v[80:87], v0\n"
        "v_smfmac_f32_16x16x64_bf16 v[12:15], v[36:39], v[88:95], v0\n"
        "s_barrier\n"
        "v_smfmac_f32_16x16x64_bf16 v[16:19], v[32:35], a[0:7], v0\n"
        "ds_read_b128 v[40:43], v1 offset:2048\n"
        "v_smfmac_f32_16x16x64_bf16 v[20:23], v[32:35], a[8:15], v0\n"
        "ds_read_b128 v[44:47], v1 offset:3072\n"
        "v_smfmac_f32_16x16x64_bf16 v[16:19], v[36:39], a[16:23], v0\n"
        "v_smfmac_f32_16x16x64_bf16 v[20:23], v[36:39], a[24:31], v0\n"
        "s_waitcnt lgkmcnt(1)\n"
        "v_smfmac_f32_16x16x64_bf16 v[8:11], v[40:43], v[96:103], v0\n"
        "v_smfmac_f32_16x16x64_bf16 v[12:15], v[40:43], v[104:111], v0\n"
        "s_waitcnt lgkmcnt(0)\n"
        "v_smfmac_f32_16x16x64_bf16 v[8:11], v[44:47], v[112:119], v0\n"
        "v_smfmac_f32_16x16x64_bf16 v[12:15], v[44:47], v[120:127], v0\n"
        "s_barrier\n"
        "v_smfmac_f32_16x16x64_bf16 v[16:19], v[40:43], a[32:39], v0\n"
        "ds_read_b128 v[48:51], v1 offset:4096\n"
        "v_smfmac_f32_16x16x64_bf16 v[20:23], v[40:43], a[40:47], v0\n"
        "ds_read_b128 v[52:55], v1 offset:5120\n"
        "v_smfmac_f32_16x16x64_bf16 v[16:19], v[44:47], a[48:55], v0\n"
        "v_add_u32 v7, s55, v1\n"
        "v_smfmac_f32_16x16x64_bf16 v[20:23], v[44:47], a[56:63], v0\n"
        "global_store_dwordx4 v1, v[60:63], s[82:83] sc1\n"
        "ds_read_b64 v[96:97], v7 offset:0\n"
        "s_waitcnt lgkmcnt(2)\n"
        "v_smfmac_f32_16x16x64_bf16 v[8:11], v[48:51], v[128:135], v0\n"
        "ds_read_b64 v[100:101], v7 offset:8\n"
        "v_smfmac_f32_16x16x64_bf16 v[12:15], v[48:51], v[136:143], v0\n"
        "ds_read_b64 v[104:105], v7 offset:1024\n"
        "s_waitcnt lgkmcnt(3)\n"
        "v_smfmac_f32_16x16x64_bf16 v[8:11], v[52:55], v[144:151], v0\n"
        "ds_read_b64 v[108:109], v7 offset:1032\n"
        "v_smfmac_f32_16x16x64_bf16 v[12:15], v[52:55], v[152:159], v0\n"
        "s_barrier\n"
        "v_smfmac_f32_16x16x64_bf16 v[16:19], v[48:51], a[64:71], v0\n"
        "ds_read_b128 v[56:59], v1 offset:6144\n"
        "v_smfmac_f32_16x16x64_bf16 v[20:23], v[48:51], a[72:79], v0\n"
        "ds_read_b128 v[60:63], v1 offset:7168\n"
        "v_smfmac_f32_16x16x64_bf16 v[16:19], v[52:55], a[80:87], v0\n"
        "ds_read_b128 v[64:67], v1 offset:8192\n"
        "v_smfmac_f32_16x16x64_bf16 v[20:23], v[52:55], a[88:95], v0\n"
        "ds_read_b128 v[68:71], v1 offset:12288\n"
        "s_waitcnt lgkmcnt(3)\n"
        "v_smfmac_f32_16x16x64_bf16 v[8:11], v[56:59], v[160:167], v0\n"
        "ds_read_b128 v[72:75], v1 offset:9216\n"
        "v_smfmac_f32_16x16x64_bf16 v[12:15], v[56:59], v[168:175], v0\n"
        "ds_read_b128 v[76:79], v1 offset:13312\n"
        "s_waitcnt lgkmcnt(4)\n"
        "v_smfmac_f32_16x16x64_bf16 v[8:11], v[60:63], v[176:183], v0\n"
        "ds_read_b128 v[80:83], v1 offset:10240\n"
        "v_smfmac_f32_16x16x64_bf16 v[12:15], v[60:63], v[184:191], v0\n"
        "ds_read_b128 v[84:87], v1 offset:14336\n"
        "v_smfmac_f32_16x16x64_bf16 v[16:19], v[56:59], a[96:103], v0\n"
        "ds_read_b128 v[88:91], v1 offset:11264\n"
        "s_nop 0\n"
        "v_smfmac_f32_16x16x64_bf16 v[20:23], v[56:59], a[104:111], v0\n"
        "ds_read_b128 v[92:95], v1 offset:15360\n"
        "s_nop 0\n"
        "v_smfmac_f32_16x16x64_bf16 v[16:19], v[60:63], a[112:119], v0\n"
        "v_add_f32 v8, v8, v9\n"
        "v_smfmac_f32_16x16x64_bf16 v[20:23], v[60:63], a[120:127], v0\n"
        "v_add_f32 v12, v12, v13\n"
        "v_add_f32 v10, v10, v11\n"
        "v_add_f32 v14, v14, v15\n"
        "v_cvt_pk_bf16_f32 v9, v8, v12\n"
        "v_cvt_pk_bf16_f32 v11, v10, v14\n"
        "ds_write2_b32 v2, v9, v11 offset1:8\n"
        "v_cmp_u_f32 vcc, v8, v10\n"
        "v_add_f32 v16, v16, v17\n"
        "v_add_f32 v20, v20, v21\n"
        "v_add_f32 v18, v18, v19\n"
        "v_add_f32 v22, v22, v23\n"
        "v_cvt_pk_bf16_f32 v99, v16, v20\n"
        "v_cvt_pk_bf16_f32 v103, v18, v22\n"
        "v_smfmac_f32_16x16x64_bf16 v[24:27], v[32:35], a[128:135], v0\n"
        "ds_write2_b32 v2, v99, v103 offset0:128 offset1:136\n"
        "v_smfmac_f32_16x16x64_bf16 v[28:31], v[32:35], a[136:143], v0\n"
        "s_cbranch_vccnz Lw0slow%=\n"
        "Lw0back%=:\n"
        "s_cmp_eq_u32 s36, 63\n"
        "s_cbranch_scc1 Llast%=\n"
        "v_smfmac_f32_16x16x64_bf16 v[24:27], v[36:39], a[144:151], v0\n"
        "v_mov_b32 v16, 0\n"
        "v_smfmac_f32_16x16x64_bf16 v[28:31], v[36:39], a[152:159], v0\n"
        "v_mov_b32 v17, 0\n"
        "v_smfmac_f32_16x16x64_bf16 v[24:27], v[40:43], a[160:167], v0\n"
        "s_waitcnt lgkmcnt(0)\n"
        "s_barrier\n"
        "ds_read_b128 v[112:115], v3 offset:24576\n"
        "v_smfmac_f32_16x16x64_bf16 v[28:31], v[40:43], a[168:175], v0\n"
        "ds_read_b128 v[116:119], v3 offset:25600\n"
        "v_mov_b32 v18, 0\n"
        "v_smfmac_f32_16x16x64_bf16 v[24:27], v[44:47], a[176:183], v0\n"
        "ds_read_b128 v[120:123], v3 offset:26624\n"
        "v_mov_b32 v19, 0\n"
        "v_smfmac_f32_16x16x64_bf16 v[28:31], v[44:47], a[184:191], v0\n"
        "ds_read_b128 v[124:127], v3 offset:27648\n"
        "v_mov_b32 v20, 0\n"
        "v_smfmac_f32_16x16x64_bf16 v[24:27], v[48:51], a[192:199], v0\n"
        "v_mov_b32 v21, 0\n"
        "s_waitcnt lgkmcnt(3)\n"
        "v_mfma_f32_16x16x32_bf16 v[8:11], v[64:67], v[112:115], 0\n"
        "ds_read_b128 v[64:67], v4 offset:25088\n"
        "v_mov_b32 v22, 0\n"
        "v_mfma_f32_16x16x32_bf16 v[12:15], v[68:71], v[112:115], 0\n"
        "ds_read_b128 v[68:71], v4 offset:26112\n"
        "v_mov_b32 v23, 0\n"
        "s_waitcnt lgkmcnt(4)\n"
        "v_mfma_f32_16x16x32_bf16 v[8:11], v[72:75], v[116:119], v[8:11]\n"
        "ds_read_b128 v[72:75], v4 offset:27136\n"
        "v_mov_b32 v32, 0x3f803f80\n"
        "v_mfma_f32_16x16x32_bf16 v[12:15], v[76:79], v[116:119], v[12:15]\n"
        "ds_read_b128 v[76:79], v4 offset:28160\n"
        "v_mov_b32 v33, 0x3f00\n"
        "s_waitcnt lgkmcnt(5)\n"
        "v_mfma_f32_16x16x32_bf16 v[8:11], v[80:83], v[120:123], v[8:11]\n"
        "v_mov_b32 v34, 0x3f803f80\n"
        "v_lshl_add_u32 v7, v1, 3, s88\n"
        "v_mfma_f32_16x16x32_bf16 v[12:15], v[84:87], v[120:123], v[12:15]\n"
        "v_mov_b32 v35, 0x3f00\n"
        "v_writelane_b32 v7, s93, 32\n"
        "s_waitcnt lgkmcnt(4)\n"
        "v_mfma_f32_16x16x32_bf16 v[8:11], v[88:91], v[124:127], v[8:11]\n"
        "v_writelane_b32 v7, s94, 33\n"
        "v_mfma_f32_16x16x32_bf16 v[12:15], v[92:95], v[124:127], v[12:15]\n"
        "s_waitcnt lgkmcnt(0)\n"
        "s_nop 1\n"
        "v_mfma_f32_16x16x32_bf16 v[80:83], v[64:67], v[112:115], 0\n"
        "s_nop 2\n"
        "v_mfma_f32_16x16x32_bf16 v[80:83], v[68:71], v[116:119], v[80:83]\n"
        "v_cndmask_b32 v8, v8, v12, s[58:59]\n"
        "v_cndmask_b32 v9, v9, v13, s[58:59]\n"
        "v_mfma_f32_16x16x32_bf16 v[80:83], v[72:75], v[120:123], v[80:83]\n"
        "v_cndmask_b32 v10, v10, v14, s[58:59]\n"
        "v_cndmask_b32 v11, v11, v15, s[58:59]\n"
        "v_mfma_f32_16x16x32_bf16 v[80:83], v[76:79], v[124:127], v[80:83]\n"
        "v_max3_f32 v12, v8, v9, v10\n"
        "v_max_f32 v12, v12, v11\n"
        "v_smfmac_f32_16x16x64_bf16 v[28:31], v[48:51], a[200:207], v0\n"
        "s_nop 0\n"
        "v_max_f32_dpp v12, v12, v12 quad_perm:[1,0,3,2] row_mask:0xf bank_mask:0xf\n"
        "v_smfmac_f32_16x16x64_bf16 v[24:27], v[52:55], a[208:215], v0\n"
        "v_mov_b32 v13, v12\n"
        "v_smfmac_f32_16x16x64_bf16 v[28:31], v[52:55], a[216:223], v0\n"
        "v_cndmask_b32 v14, v80, v81, s[58:59]\n"
        "v_permlane32_swap_b32 v12, v13\n"
        "v_max3_f32 v12, v12, v13, v80\n"
        "v_smfmac_f32_16x16x64_bf16 v[24:27], v[56:59], a[224:231], v0\n"
        "v_max3_f32 v12, v12, v81, v82\n"
        "v_max_f32 v13, v12, v83\n"
        "v_max_f32 v12, v12, v83\n"
        "v_smfmac_f32_16x16x64_bf16 v[28:31], v[56:59], a[232:239], v0\n"
        "v_cndmask_b32 v15, v82, v83, s[58:59]\n"
        "v_permlane16_swap_b32 v12, v13\n"
        "v_smfmac_f32_16x16x64_bf16 v[24:27], v[60:63], a[240:247], v0\n"
        "v_max_f32 v12, v12, v13\n"
        "v_smfmac_f32_16x16x64_bf16 v[28:31], v[60:63], a[248:255], v0\n"
        "v_mul_f32 v12, s60, v12\n"
        "v_fma_f32 v8, v8, s60, -v12\n"
        "v_fma_f32 v9, v9, s60, -v12\n"
        "v_fma_f32 v10, v10, s60, -v12\n"
        "v_fma_f32 v11, v11, s60, -v12\n"
        "v_fma_f32 v14, v14, s60, -v12\n"
        "v_fma_f32 v15, v15, s60, -v12\n"
        "v_exp_f32 v8, v8\n"
        "v_exp_f32 v9, v9\n"
        "v_exp_f32 v10, v10\n"
        "v_exp_f32 v11, v11\n"
        "v_exp_f32 v14, v14\n"
        "v_exp_f32 v15, v15\n"
        "v_cvt_pk_bf16_f32 v36, v8, v9\n"
        "v_cvt_pk_bf16_f32 v38, v10, v11\n"
        "v_cvt_pk_bf16_f32 v37, v14, 0\n"
        "v_cvt_pk_bf16_f32 v39, v15, 0\n"
        "v_cvt_pk_bf16_f32 v98, v24, v26\n"
        "v_cvt_pk_bf16_f32 v102, v25, v27\n"
        "v_mfma_f32_16x16x32_bf16 v[84:87], v[36:39], v[32:35], 0\n"
        "v_cvt_pk_bf16_f32 v106, v28, v30\n"
        "v_cvt_pk_bf16_f32 v110, v29, v31\n"
        "v_smfmac_f32_16x16x64_bf16 v[16:19], v[36:39], v[96:103], v0 abid:1\n"
        "v_add_u32 v9, 4096, v2\n"
        "v_smfmac_f32_16x16x64_bf16 v[20:23], v[36:39], v[104:111], v0 abid:1\n"
        "s_nop 1\n"
        "v_mov_b32 v61, 0\n"
        "v_add_f32 v84, v84, v85\n"
        "v_add_f32 v86, v86, v87\n"
        "v_rcp_f32 v84, v84\n"
        "v_rcp_f32 v86, v86\n"
        "v_add_f32 v16, v16, v17\n"
        "v_add_f32 v20, v20, v21\n"
        "v_add_f32 v18, v18, v19\n"
        "v_mul_f32 v16, v16, v84\n"
        "v_mul_f32 v20, v20, v84\n"
        "v_add_f32 v22, v22, v23\n"
        "v_cvt_pk_bf16_f32 v17, v16, v20\n"
        "v_mul_f32 v18, v18, v86\n"
        "v_mul_f32 v22, v22, v86\n"
        "v_cvt_pk_bf16_f32 v19, v18, v22\n"
        "ds_write2_b32 v9, v17, v19 offset1:8\n"
        "v_mov_b32 v60, 0\n"
        "v_mov_b32 v62, 0\n"
        "v_mov_b32 v63, 0\n"
        "v_add_f32 v10, v24, v25\n"
        "v_add_f32 v11, v26, v27\n"
        "v_mfma_f32_16x16x32_bf16 v[32:35], v[60:63], v[60:63], 0\n"
        "v_add_f32 v16, v28, v29\n"
        "v_add_f32 v18, v30, v31\n"
        "v_mfma_f32_16x16x32_bf16 v[36:39], v[60:63], v[60:63], 0\n"
        "v_mfma_f32_16x16x32_bf16 v[40:43], v[60:63], v[60:63], 0\n"
        "s_waitcnt lgkmcnt(0)\n"
        "s_barrier\n"
        "ds_read_b128 v[24:27], v1 offset:28672\n"
        "v_mfma_f32_16x16x32_bf16 v[44:47], v[60:63], v[60:63], 0\n"
        "ds_read_b128 v[28:31], v1 offset:29696\n"
        "v_add_u32 v8, 0x10000, v6\n"
        "v_mov_b32 v12, v99\n"
        "v_mfma_f32_16x16x32_bf16 v[48:51], v[60:63], v[60:63], 0\n"
        "ds_read_b128 v[64:67], v8 offset:0\n"
        "v_mov_b32 v13, v103\n"
        "v_cvt_pk_bf16_f32 v14, v10, v11\n"
        "v_mfma_f32_16x16x32_bf16 v[52:55], v[60:63], v[60:63], 0\n"
        "ds_read_b128 v[68:71], v8 offset:1024\n"
        "v_cvt_pk_bf16_f32 v15, v16, v18\n"
        "s_mov_b64 exec, s[90:91]\n"
        "global_load_dword v7, v7, s[56:57] sc1\n"
        "s_mov_b64 exec, -1\n"
        "s_waitcnt lgkmcnt(3)\n"
        "v_smfmac_f32_16x16x64_bf16 v[32:35], v[24:27], v[192:199], v0\n"
        "ds_read_b128 v[72:75], v8 offset:2048\n"
        "v_smfmac_f32_16x16x64_bf16 v[36:39], v[24:27], v[200:207], v0\n"
        "ds_read_b128 v[76:79], v8 offset:3072\n"
        "s_waitcnt lgkmcnt(4)\n"
        "v_smfmac_f32_16x16x64_bf16 v[32:35], v[28:31], v[208:215], v0\n"
        "ds_read_b128 v[80:83], v8 offset:16384\n"
        "v_mov_b32 v56, 0\n"
        "v_smfmac_f32_16x16x64_bf16 v[36:39], v[28:31], v[216:223], v0\n"
        "ds_read_b128 v[84:87], v8 offset:17408\n"
        "s_nop 0\n"
        "v_mov_b32 v57, 0\n"
        "v_smfmac_f32_16x16x64_bf16 v[40:43], v[24:27], v[224:231], v0\n"
        "ds_read_b128 v[88:91], v8 offset:18432\n"
        "s_nop 0\n"
        "v_add_f32 v32, v32, v33\n"
        "v_add_f32 v34, v34, v35\n"
        "v_add_f32 v36, v36, v37\n"
        "v_add_f32 v38, v38, v39\n"
        "v_cvt_pk_bf16_f32 v32, v32, v36\n"
        "v_cvt_pk_bf16_f32 v33, v34, v38\n"
        "v_smfmac_f32_16x16x64_bf16 v[44:47], v[24:27], v[232:239], v0\n"
        "global_store_dwordx2 v5, v[32:33], s[44:45] sc1\n"
        "v_smfmac_f32_16x16x64_bf16 v[40:43], v[28:31], v[240:247], v0\n"
        "ds_read_b128 v[92:95], v8 offset:19456\n"
        "ds_read_b128 v[96:99], v8 offset:32768\n"
        "v_smfmac_f32_16x16x64_bf16 v[44:47], v[28:31], v[248:255], v0\n"
        "ds_read_b128 v[100:103], v8 offset:33792\n"
        "s_waitcnt lgkmcnt(6)\n"
        "v_mov_b32 v58, 0\n"
        "v_smfmac_f32_16x16x64_bf16 v[48:51], v[24:27], v[64:71], v0\n"
        "ds_read_b128 v[104:107], v8 offset:34816\n"
        "s_nop 0\n"
        "v_add_f32 v40, v40, v41\n"
        "v_add_f32 v42, v42, v43\n"
        "v_add_f32 v44, v44, v45\n"
        "v_add_f32 v46, v46, v47\n"
        "v_cvt_pk_bf16_f32 v40, v40, v44\n"
        "v_cvt_pk_bf16_f32 v41, v42, v46\n"
        "v_smfmac_f32_16x16x64_bf16 v[52:55], v[24:27], v[72:79], v0\n"
        "global_store_dwordx2 v5, v[40:41], s[46:47] sc1\n"
        "s_waitcnt lgkmcnt(3)\n"
        "v_smfmac_f32_16x16x64_bf16 v[48:51], v[28:31], v[80:87], v0\n"
        "ds_read_b128 v[108:111], v8 offset:35840\n"
        "ds_read_b128 v[112:115], v8 offset:49152\n"
        "v_mov_b32 v59, 0\n"
        "v_smfmac_f32_16x16x64_bf16 v[52:55], v[28:31], v[88:95], v0\n"
        "ds_read_b128 v[116:119], v8 offset:50176\n"
        "s_waitcnt lgkmcnt(4)\n"
        "s_nop 0\n"
        "v_smfmac_f32_16x16x64_bf16 v[56:59], v[24:27], v[96:103], v0\n"
        "ds_read_b128 v[120:123], v8 offset:51200\n"
        "ds_read_b128 v[124:127], v8 offset:52224\n"
        "s_waitcnt lgkmcnt(4)\n"
        "v_add_f32 v48, v48, v49\n"
        "v_add_f32 v50, v50, v51\n"
        "v_add_f32 v52, v52, v53\n"
        "v_add_f32 v54, v54, v55\n"
        "v_cvt_pk_bf16_f32 v48, v48, v52\n"
        "v_cvt_pk_bf16_f32 v49, v50, v54\n"
        "v_smfmac_f32_16x16x64_bf16 v[60:63], v[24:27], v[104:111], v0\n"
        "global_store_dwordx2 v5, v[48:49], s[48:49] sc1\n"
        "s_waitcnt lgkmcnt(0)\n"
        "v_smfmac_f32_16x16x64_bf16 v[56:59], v[28:31], v[112:119], v0\n"
        "v_smfmac_f32_16x16x64_bf16 v[60:63], v[28:31], v[120:127], v0\n"
        "s_nop 6\n"
        "v_add_f32 v56, v56, v57\n"
        "v_add_f32 v58, v58, v59\n"
        "v_add_f32 v60, v60, v61\n"
        "v_add_f32 v62, v62, v63\n"
        "v_cvt_pk_bf16_f32 v56, v56, v60\n"
        "v_cvt_pk_bf16_f32 v57, v58, v62\n"
        "global_store_dwordx2 v5, v[56:57], s[50:51] sc1\n"
        "s_memrealtime s[0:1]\n"
        "s_nop 1\n"
        "s_memrealtime s[2:3]\n"
        "s_nop 1\n"
        "s_memrealtime s[4:5]\n"
        "s_nop 1\n"
        "s_memrealtime s[6:7]\n"
        "s_nop 1\n"
        "s_memrealtime s[8:9]\n"
        "s_nop 1\n"
        "s_memrealtime s[10:11]\n"
        "s_nop 1\n"
        "s_memrealtime s[12:13]\n"
        "s_nop 1\n"
        "s_memrealtime s[14:15]\n"
        "s_memtime s[68:69]\n"
        "v_mov_b32 v59, s36\n"
        "s_add_u32 s36, s36, 1\n"
        "s_waitcnt vmcnt(4) lgkmcnt(0)\n"
        "s_add_u32 s73, s0, s2\n"
        "s_add_u32 s73, s73, s4\n"
        "s_add_u32 s73, s73, s6\n"
        "s_add_u32 s73, s73, s8\n"
        "s_add_u32 s73, s73, s10\n"
        "s_add_u32 s73, s73, s12\n"
        "s_add_u32 s73, s73, s14\n"
        "Lchk%=:\n"
        "s_add_u32 s66, s36, 2\n"
        "s_min_u32 s66, s66, 64\n"
        "v_cmp_gt_u32 s[70:71], s66, v7\n"
        "v_and_b32 v32, 0xff, v7\n"
        "v_cmp_gt_u32 vcc, s36, v32\n"
        "s_and_b32 s71, s71, 3\n"
        "s_or_b32 s65, s71, vcc_lo\n"
        "s_cbranch_scc1 Lrepoll%=\n"
        "v_readlane_b32 s65, v7, 34\n"
        "s_cmp_eq_u32 s65, 0\n"
        "s_cselect_b32 s95, s95, s65\n"
        "s_lshl_b32 s65, s73, 8\n"
        "v_cmp_eq_u32 vcc, s36, v32\n"
        "v_subrev_u32 v7, s65, v7\n"
        "v_bfrev_b32 v32, 1\n"
        "v_cndmask_b32 v7, v32, v7, vcc\n"
        "s_nop 1\n"
        "v_max_i32_dpp v7, v7, v7 row_ror:8 row_mask:0xf bank_mask:0xf\n"
        "s_nop 1\n"
        "v_max_i32_dpp v7, v7, v7 row_ror:4 row_mask:0xf bank_mask:0xf\n"
        "s_nop 1\n"
        "v_max_i32_dpp v7, v7, v7 row_ror:2 row_mask:0xf bank_mask:0xf\n"
        "s_nop 1\n"
        "v_max_i32_dpp v7, v7, v7 row_ror:1 row_mask:0xf bank_mask:0xf\n"
        "s_nop 0\n"
        "v_readlane_b32 s64, v7, 0\n"
        "v_readlane_b32 s65, v7, 16\n"
        "s_max_i32 s64, s64, s65\n"
        "s_ashr_i32 s64, s64, 8\n"
        "s_add_u32 s64, s64, s73\n"
        "s_sub_u32 s65, s73, s62\n"
        "s_ashr_i32 s66, s65, s33\n"
        "s_max_i32 s65, s65, s66\n"
        "s_min_i32 s65, s65, 30\n"
        "s_cmp_eq_u32 s63, 0\n"
        "s_cselect_b32 s65, s65, 0\n"
        "s_cselect_b32 s33, 2, s33\n"
        "s_add_u32 s61, s61, s65\n"
        "s_mov_b32 s63, s32\n"
        "s_mov_b32 s32, 0\n"
        "s_sub_u32 s65, s64, s73\n"
        "s_max_i32 s65, s65, -40\n"
        "s_add_u32 s64, s73, s65\n"
        "s_add_u32 s64, s64, s95\n"
        "s_add_u32 s64, s64, s72\n"
        "s_mov_b32 s72, 0\n"
        "s_add_u32 s62, s64, s61\n"
        "s_add_u32 s65, s62, s63\n"
        "s_lshl_b32 s65, s65, 8\n"
        "s_add_u32 s65, s65, s36\n"
        "v_add_u32 v33, s65, 1\n"
        "s_mov_b64 exec, 1\n"
        "global_store_dword v1, v33, s[78:79] sc1\n"
        "s_mov_b64 exec, -1\n"
        "s_mov_b32 s63, 0\n"
        "s_mov_b32 m0, s37\n"
        "s_nop 0\n"
        "global_load_lds_dwordx4 v1, s[38:39] sc1\n"
        "global_load_lds_dwordx4 v1, s[38:39] offset:1024 sc1\n"
        "s_mov_b32 m0, s55\n"
        "s_nop 0\n"
        "global_load_lds_dwordx4 v1, s[40:41] sc1\n"
        "global_load_lds_dwordx4 v1, s[40:41] offset:1024 sc1\n"
        "s_mov_b64 s[96:97], s[52:53]\n"
        "s_add_u32 s54, s54, 1\n"
        "s_cmp_eq_u32 s54, 3\n"
        "s_cselect_b32 s54, 0, s54\n"
        "s_cselect_b32 s65, 0xfffe0000, s98\n"
        "s_add_u32 s52, s52, s65\n"
        "s_mov_b32 m0, 0\n"
        "s_sub_u32 s65, s64, s73\n"
        "s_cmp_gt_i32 s65, 300\n"
        "s_cbranch_scc1 Llong%=\n"
        "Lshort%=:\n"
        "s_sub_u32 s65, s64, s73\n"
        "s_mul_i32 s65, s65, 3\n"
        "s_add_u32 s65, s65, s68\n"
        "Lpba%=:\n"
        "s_memtime s[70:71]\n"
        "s_getpc_b64 s[74:75]\n"
        "Lpbb%=:\n"
        "s_waitcnt lgkmcnt(0)\n"
        "s_sub_u32 s66, s65, s70\n"
        "s_cmp_gt_i32 s66, 127\n"
        "s_cbranch_scc1 Lpba%=\n"
        "s_max_i32 s66, s66, 0\n"
        "s_andn2_b32 s66, s66, 3\n"
        "s_sub_u32 s66, Lpbc%=-Lpbb%=, s66\n"
        "s_add_u32 s74, s74, s66\n"
        "s_addc_u32 s75, s75, 0\n"
        "s_setpc_b64 s[74:75]\n"
        "s_nop 0\n"
        "s_nop 0\n"
        "s_nop 0\n"
        "s_nop 0\n"
        "s_nop 0\n"
        "s_nop 0\n"
        "s_nop 0\n"
        "s_nop 0\n"
        "s_nop 0\n"
        "s_nop 0\n"
        "s_nop 0\n"
        "s_nop 0\n"
        "s_nop 0\n"
        "s_nop 0\n"
        "s_nop 0\n"
        "s_nop 0\n"
        "s_nop 0\n"
        "s_nop 0\n"
        "s_nop 0\n"
        "s_nop 0\n"
        "s_nop 0\n"
        "s_nop 0\n"
        "s_nop 0\n"
        "s_nop 0\n"
        "s_nop 0\n"
        "s_nop 0\n"
        "s_nop 0\n"
        "s_nop 0\n"
        "s_nop 0\n"
        "s_nop 0\n"
        "s_nop 0\n"
        "Lpbc%=:\n"
        "s_barrier\n"
        "global_load_lds_dwordx4 v1, s[96:97] sc1\n"
        "global_load_lds_dwordx4 v1, s[96:97] offset:1024 sc1\n"
        "ds_read_b128 v[64:67], v6 offset:0\n"
        "ds_read_b128 v[68:71], v6 offset:1024\n"
        "ds_read_b128 v[72:75], v6 offset:2048\n"
        "ds_read_b128 v[76:79], v6 offset:3072\n"
        "ds_read_b128 v[80:83], v6 offset:16384\n"
        "ds_read_b128 v[84:87], v6 offset:17408\n"
        "ds_read_b128 v[88:91], v6 offset:18432\n"
        "ds_read_b128 v[92:95], v6 offset:19456\n"
        "ds_read_b128 v[96:99], v6 offset:32768\n"
        "ds_read_b128 v[100:103], v6 offset:33792\n"
        "ds_read_b128 v[104:107], v6 offset:34816\n"
        "ds_read_b128 v[108:111], v6 offset:35840\n"
        "ds_read_b128 v[112:115], v6 offset:49152\n"
        "ds_read_b128 v[116:119], v6 offset:50176\n"
        "ds_read_b128 v[120:123], v6 offset:51200\n"
        "ds_read_b128 v[124:127], v6 offset:52224\n"
        "v_mov_b32 v60, v12\n"
        "v_mov_b32 v61, v13\n"
        "v_mov_b32 v62, v14\n"
        "v_mov_b32 v63, v15\n"
        "v_mov_b32 v8, 0\n"
        "v_mov_b32 v9, 0\n"
        "v_mov_b32 v10, 0\n"
        "v_mov_b32 v11, 0\n"
        "v_mov_b32 v12, 0\n"
        "v_mov_b32 v13, 0\n"
        "v_mov_b32 v14, 0\n"
        "v_mov_b32 v15, 0\n"
        "v_mov_b32 v16, 0\n"
        "v_mov_b32 v17, 0\n"
        "v_mov_b32 v18, 0\n"
        "v_mov_b32 v19, 0\n"
        "v_mov_b32 v20, 0\n"
        "v_mov_b32 v21, 0\n"
        "v_mov_b32 v22, 0\n"
        "v_mov_b32 v23, 0\n"
        "v_mov_b32 v24, 0\n"
        "v_mov_b32 v25, 0\n"
        "v_mov_b32 v26, 0\n"
        "v_mov_b32 v27, 0\n"
        "v_mov_b32 v28, 0\n"
        "v_mov_b32 v29, 0\n"
        "v_mov_b32 v30, 0\n"
        "v_mov_b32 v31, 0\n"
        "s_xor_b32 s44, s44, 0x100000\n"
        "s_xor_b32 s46, s46, 0x100000\n"
        "s_xor_b32 s48, s48, 0x100000\n"
        "s_xor_b32 s50, s50, 0x100000\n"
        "s_add_u32 s38, s38, 0x40000\n"
        "s_addc_u32 s39, s39, 0\n"
        "s_add_u32 s40, s40, 0x40000\n"
        "s_addc_u32 s41, s41, 0\n"
        "s_mov_b64 s[82:83], s[42:43]\n"
        "s_add_u32 s42, s42, 0x20000\n"
        "s_addc_u32 s43, s43, 0\n"
        "s_waitcnt lgkmcnt(0)\n"
        "s_branch Lw0step%=\n"
        "Lw1step%=:\n"
        "s_barrier\n"
        "ds_read_b128 v[32:35], v1 offset:0\n"
        "ds_read_b128 v[36:39], v1 offset:1024\n"
        "s_waitcnt lgkmcnt(1)\n"
        "v_smfmac_f32_16x16x64_bf16 v[8:11], v[32:35], v[64:71], v0\n"
        "v_smfmac_f32_16x16x64_bf16 v[12:15], v[32:35], v[72:79], v0\n"
        "s_waitcnt lgkmcnt(0)\n"
        "v_smfmac_f32_16x16x64_bf16 v[8:11], v[36:39], v[80:87], v0\n"
        "v_smfmac_f32_16x16x64_bf16 v[12:15], v[36:39], v[88:95], v0\n"
        "s_waitcnt vmcnt(0)\n"
        "s_barrier\n"
        "v_smfmac_f32_16x16x64_bf16 v[16:19], v[32:35], a[0:7], v0\n"
        "ds_read_b128 v[40:43], v1 offset:2048\n"
        "v_smfmac_f32_16x16x64_bf16 v[20:23], v[32:35], a[8:15], v0\n"
        "ds_read_b128 v[44:47], v1 offset:3072\n"
        "v_smfmac_f32_16x16x64_bf16 v[16:19], v[36:39], a[16:23], v0\n"
        "v_smfmac_f32_16x16x64_bf16 v[20:23], v[36:39], a[24:31], v0\n"
        "s_waitcnt lgkmcnt(1)\n"
        "v_smfmac_f32_16x16x64_bf16 v[8:11], v[40:43], v[96:103], v0\n"
        "v_smfmac_f32_16x16x64_bf16 v[12:15], v[40:43], v[104:111], v0\n"
        "s_waitcnt lgkmcnt(0)\n"
        "v_smfmac_f32_16x16x64_bf16 v[8:11], v[44:47], v[112:119], v0\n"
        "v_smfmac_f32_16x16x64_bf16 v[12:15], v[44:47], v[120:127], v0\n"
        "s_barrier\n"
        "v_smfmac_f32_16x16x64_bf16 v[16:19], v[40:43], a[32:39], v0\n"
        "ds_read_b128 v[48:51], v1 offset:4096\n"
        "v_smfmac_f32_16x16x64_bf16 v[20:23], v[40:43], a[40:47], v0\n"
        "ds_read_b128 v[52:55], v1 offset:5120\n"
        "v_smfmac_f32_16x16x64_bf16 v[16:19], v[44:47], a[48:55], v0\n"
        "v_add_u32 v7, s55, v1\n"
        "v_smfmac_f32_16x16x64_bf16 v[20:23], v[44:47], a[56:63], v0\n"
        "ds_read_b64 v[96:97], v7 offset:0\n"
        "s_waitcnt lgkmcnt(2)\n"
        "v_smfmac_f32_16x16x64_bf16 v[8:11], v[48:51], v[128:135], v0\n"
        "global_store_dwordx4 v1, v[60:63], s[82:83] sc1\n"
        "ds_read_b64 v[100:101], v7 offset:8\n"
        "v_smfmac_f32_16x16x64_bf16 v[12:15], v[48:51], v[136:143], v0\n"
        "ds_read_b64 v[104:105], v7 offset:1024\n"
        "s_waitcnt lgkmcnt(3)\n"
        "v_smfmac_f32_16x16x64_bf16 v[8:11], v[52:55], v[144:151], v0\n"
        "ds_read_b64 v[108:109], v7 offset:1032\n"
        "v_smfmac_f32_16x16x64_bf16 v[12:15], v[52:55], v[152:159], v0\n"
        "s_barrier\n"
        "v_smfmac_f32_16x16x64_bf16 v[16:19], v[48:51], a[64:71], v0\n"
        "ds_read_b128 v[56:59], v1 offset:6144\n"
        "v_smfmac_f32_16x16x64_bf16 v[20:23], v[48:51], a[72:79], v0\n"
        "ds_read_b128 v[60:63], v1 offset:7168\n"
        "v_smfmac_f32_16x16x64_bf16 v[16:19], v[52:55], a[80:87], v0\n"
        "ds_read_b128 v[64:67], v1 offset:8192\n"
        "v_smfmac_f32_16x16x64_bf16 v[20:23], v[52:55], a[88:95], v0\n"
        "ds_read_b128 v[68:71], v1 offset:12288\n"
        "s_waitcnt lgkmcnt(3)\n"
        "v_smfmac_f32_16x16x64_bf16 v[8:11], v[56:59], v[160:167], v0\n"
        "ds_read_b128 v[72:75], v1 offset:9216\n"
        "v_smfmac_f32_16x16x64_bf16 v[12:15], v[56:59], v[168:175], v0\n"
        "ds_read_b128 v[76:79], v1 offset:13312\n"
        "s_waitcnt lgkmcnt(4)\n"
        "v_smfmac_f32_16x16x64_bf16 v[8:11], v[60:63], v[176:183], v0\n"
        "ds_read_b128 v[80:83], v1 offset:10240\n"
        "v_smfmac_f32_16x16x64_bf16 v[12:15], v[60:63], v[184:191], v0\n"
        "ds_read_b128 v[84:87], v1 offset:14336\n"
        "v_smfmac_f32_16x16x64_bf16 v[16:19], v[56:59], a[96:103], v0\n"
        "ds_read_b128 v[88:91], v1 offset:11264\n"
        "s_nop 0\n"
        "v_smfmac_f32_16x16x64_bf16 v[20:23], v[56:59], a[104:111], v0\n"
        "ds_read_b128 v[92:95], v1 offset:15360\n"
        "s_nop 0\n"
        "v_smfmac_f32_16x16x64_bf16 v[16:19], v[60:63], a[112:119], v0\n"
        "v_add_f32 v8, v8, v9\n"
        "v_smfmac_f32_16x16x64_bf16 v[20:23], v[60:63], a[120:127], v0\n"
        "v_add_f32 v12, v12, v13\n"
        "v_add_f32 v10, v10, v11\n"
        "v_add_f32 v14, v14, v15\n"
        "v_cvt_pk_bf16_f32 v9, v8, v12\n"
        "v_cvt_pk_bf16_f32 v11, v10, v14\n"
        "ds_write2_b32 v2, v9, v11 offset1:8\n"
        "v_cmp_u_f32 vcc, v8, v10\n"
        "v_add_f32 v16, v16, v17\n"
        "v_add_f32 v20, v20, v21\n"
        "v_add_f32 v18, v18, v19\n"
        "v_add_f32 v22, v22, v23\n"
        "v_cvt_pk_bf16_f32 v99, v16, v20\n"
        "v_cvt_pk_bf16_f32 v103, v18, v22\n"
        "v_smfmac_f32_16x16x64_bf16 v[24:27], v[32:35], a[128:135], v0\n"
        "ds_write2_b32 v2, v99, v103 offset0:128 offset1:136\n"
        "v_smfmac_f32_16x16x64_bf16 v[28:31], v[32:35], a[136:143], v0\n"
        "s_cbranch_vccnz Lw1slow%=\n"
        "Lw1back%=:\n"
        "s_cmp_eq_u32 s36, 63\n"
        "s_cbranch_scc1 Llast%=\n"
        "v_smfmac_f32_16x16x64_bf16 v[24:27], v[36:39], a[144:151], v0\n"
        "v_mov_b32 v16, 0\n"
        "v_smfmac_f32_16x16x64_bf16 v[28:31], v[36:39], a[152:159], v0\n"
        "v_mov_b32 v17, 0\n"
        "v_smfmac_f32_16x16x64_bf16 v[24:27], v[40:43], a[160:167], v0\n"
        "s_waitcnt lgkmcnt(0)\n"
        "s_barrier\n"
        "ds_read_b128 v[112:115], v3 offset:24576\n"
        "v_smfmac_f32_16x16x64_bf16 v[28:31], v[40:43], a[168:175], v0\n"
        "ds_read_b128 v[116:119], v3 offset:25600\n"
        "v_mov_b32 v18, 0\n"
        "v_smfmac_f32_16x16x64_bf16 v[24:27], v[44:47], a[176:183], v0\n"
        "ds_read_b128 v[120:123], v3 offset:26624\n"
        "v_mov_b32 v19, 0\n"
        "v_smfmac_f32_16x16x64_bf16 v[28:31], v[44:47], a[184:191], v0\n"
        "ds_read_b128 v[124:127], v3 offset:27648\n"
        "v_mov_b32 v20, 0\n"
        "v_smfmac_f32_16x16x64_bf16 v[24:27], v[48:51], a[192:199], v0\n"
        "v_mov_b32 v21, 0\n"
        "s_waitcnt lgkmcnt(3)\n"
        "v_mfma_f32_16x16x32_bf16 v[8:11], v[64:67], v[112:115], 0\n"
        "ds_read_b128 v[64:67], v4 offset:25088\n"
        "v_mov_b32 v22, 0\n"
        "v_mfma_f32_16x16x32_bf16 v[12:15], v[68:71], v[112:115], 0\n"
        "ds_read_b128 v[68:71], v4 offset:26112\n"
        "v_mov_b32 v23, 0\n"
        "s_waitcnt lgkmcnt(4)\n"
        "v_mfma_f32_16x16x32_bf16 v[8:11], v[72:75], v[116:119], v[8:11]\n"
        "ds_read_b128 v[72:75], v4 offset:27136\n"
        "v_mov_b32 v32, 0x3f803f80\n"
        "v_mfma_f32_16x16x32_bf16 v[12:15], v[76:79], v[116:119], v[12:15]\n"
        "ds_read_b128 v[76:79], v4 offset:28160\n"
        "v_mov_b32 v33, 0x3f00\n"
        "s_waitcnt lgkmcnt(5)\n"
        "v_mfma_f32_16x16x32_bf16 v[8:11], v[80:83], v[120:123], v[8:11]\n"
        "v_mov_b32 v34, 0x3f803f80\n"
        "v_mfma_f32_16x16x32_bf16 v[12:15], v[84:87], v[120:123], v[12:15]\n"
        "v_mov_b32 v35, 0x3f00\n"
        "s_waitcnt lgkmcnt(4)\n"
        "v_mfma_f32_16x16x32_bf16 v[8:11], v[88:91], v[124:127], v[8:11]\n"
        "v_mfma_f32_16x16x32_bf16 v[12:15], v[92:95], v[124:127], v[12:15]\n"
        "s_waitcnt lgkmcnt(0)\n"
        "s_nop 1\n"
        "v_mfma_f32_16x16x32_bf16 v[80:83], v[64:67], v[112:115], 0\n"
        "s_nop 2\n"
        "v_mfma_f32_16x16x32_bf16 v[80:83], v[68:71], v[116:119], v[80:83]\n"
        "v_cndmask_b32 v8, v8, v12, s[58:59]\n"
        "v_cndmask_b32 v9, v9, v13, s[58:59]\n"
        "v_mfma_f32_16x16x32_bf16 v[80:83], v[72:75], v[120:123], v[80:83]\n"
        "v_cndmask_b32 v10, v10, v14, s[58:59]\n"
        "v_cndmask_b32 v11, v11, v15, s[58:59]\n"
        "v_mfma_f32_16x16x32_bf16 v[80:83], v[76:79], v[124:127], v[80:83]\n"
        "v_max3_f32 v12, v8, v9, v10\n"
        "v_max_f32 v12, v12, v11\n"
        "v_smfmac_f32_16x16x64_bf16 v[28:31], v[48:51], a[200:207], v0\n"
        "s_nop 0\n"
        "v_max_f32_dpp v12, v12, v12 quad_perm:[1,0,3,2] row_mask:0xf bank_mask:0xf\n"
        "v_smfmac_f32_16x16x64_bf16 v[24:27], v[52:55], a[208:215], v0\n"
        "v_mov_b32 v13, v12\n"
        "v_smfmac_f32_16x16x64_bf16 v[28:31], v[52:55], a[216:223], v0\n"
        "v_cndmask_b32 v14, v80, v81, s[58:59]\n"
        "v_permlane32_swap_b32 v12, v13\n"
        "v_max3_f32 v12, v12, v13, v80\n"
        "v_smfmac_f32_16x16x64_bf16 v[24:27], v[56:59], a[224:231], v0\n"
        "v_max3_f32 v12, v12, v81, v82\n"
        "v_max_f32 v13, v12, v83\n"
        "v_max_f32 v12, v12, v83\n"
        "v_smfmac_f32_16x16x64_bf16 v[28:31], v[56:59], a[232:239], v0\n"
        "v_cndmask_b32 v15, v82, v83, s[58:59]\n"
        "v_permlane16_swap_b32 v12, v13\n"
        "v_smfmac_f32_16x16x64_bf16 v[24:27], v[60:63], a[240:247], v0\n"
        "v_max_f32 v12, v12, v13\n"
        "v_smfmac_f32_16x16x64_bf16 v[28:31], v[60:63], a[248:255], v0\n"
        "v_mul_f32 v12, s60, v12\n"
        "v_fma_f32 v8, v8, s60, -v12\n"
        "v_fma_f32 v9, v9, s60, -v12\n"
        "v_fma_f32 v10, v10, s60, -v12\n"
        "v_fma_f32 v11, v11, s60, -v12\n"
        "v_fma_f32 v14, v14, s60, -v12\n"
        "v_fma_f32 v15, v15, s60, -v12\n"
        "v_exp_f32 v8, v8\n"
        "v_exp_f32 v9, v9\n"
        "v_exp_f32 v10, v10\n"
        "v_exp_f32 v11, v11\n"
        "v_exp_f32 v14, v14\n"
        "v_exp_f32 v15, v15\n"
        "v_cvt_pk_bf16_f32 v36, v8, v9\n"
        "v_cvt_pk_bf16_f32 v38, v10, v11\n"
        "v_cvt_pk_bf16_f32 v37, v14, 0\n"
        "v_cvt_pk_bf16_f32 v39, v15, 0\n"
        "v_cvt_pk_bf16_f32 v98, v24, v26\n"
        "v_cvt_pk_bf16_f32 v102, v25, v27\n"
        "v_mfma_f32_16x16x32_bf16 v[84:87], v[36:39], v[32:35], 0\n"
        "v_cvt_pk_bf16_f32 v106, v28, v30\n"
        "v_cvt_pk_bf16_f32 v110, v29, v31\n"
        "v_smfmac_f32_16x16x64_bf16 v[16:19], v[36:39], v[96:103], v0 abid:1\n"
        "v_add_u32 v9, 3584, v2\n"
        "v_smfmac_f32_16x16x64_bf16 v[20:23], v[36:39], v[104:111], v0 abid:1\n"
        "s_nop 1\n"
        "v_mov_b32 v61, 0\n"
        "v_add_f32 v84, v84, v85\n"
        "v_add_f32 v86, v86, v87\n"
        "v_rcp_f32 v84, v84\n"
        "v_rcp_f32 v86, v86\n"
        "v_add_f32 v16, v16, v17\n"
        "v_add_f32 v20, v20, v21\n"
        "v_add_f32 v18, v18, v19\n"
        "v_mul_f32 v16, v16, v84\n"
        "v_mul_f32 v20, v20, v84\n"
        "v_add_f32 v22, v22, v23\n"
        "v_cvt_pk_bf16_f32 v17, v16, v20\n"
        "v_mul_f32 v18, v18, v86\n"
        "v_mul_f32 v22, v22, v86\n"
        "v_cvt_pk_bf16_f32 v19, v18, v22\n"
        "ds_write2_b32 v9, v17, v19 offset1:8\n"
        "v_mov_b32 v60, 0\n"
        "v_mov_b32 v62, 0\n"
        "v_mov_b32 v63, 0\n"
        "v_add_f32 v10, v24, v25\n"
        "v_add_f32 v11, v26, v27\n"
        "v_mfma_f32_16x16x32_bf16 v[32:35], v[60:63], v[60:63], 0\n"
        "v_add_f32 v16, v28, v29\n"
        "v_add_f32 v18, v30, v31\n"
        "v_mfma_f32_16x16x32_bf16 v[36:39], v[60:63], v[60:63], 0\n"
        "v_mfma_f32_16x16x32_bf16 v[40:43], v[60:63], v[60:63], 0\n"
        "s_waitcnt lgkmcnt(0)\n"
        "s_barrier\n"
        "ds_read_b128 v[24:27], v1 offset:28672\n"
        "v_mfma_f32_16x16x32_bf16 v[44:47], v[60:63], v[60:63], 0\n"
        "ds_read_b128 v[28:31], v1 offset:29696\n"
        "v_add_u32 v8, 0x10000, v6\n"
        "v_mov_b32 v12, v99\n"
        "v_mfma_f32_16x16x32_bf16 v[48:51], v[60:63], v[60:63], 0\n"
        "ds_read_b128 v[64:67], v8 offset:0\n"
        "v_mov_b32 v13, v103\n"
        "v_cvt_pk_bf16_f32 v14, v10, v11\n"
        "v_mfma_f32_16x16x32_bf16 v[52:55], v[60:63], v[60:63], 0\n"
        "ds_read_b128 v[68:71], v8 offset:1024\n"
        "v_cvt_pk_bf16_f32 v15, v16, v18\n"
        "s_waitcnt lgkmcnt(3)\n"
        "v_smfmac_f32_16x16x64_bf16 v[32:35], v[24:27], v[192:199], v0\n"
        "ds_read_b128 v[72:75], v8 offset:2048\n"
        "v_smfmac_f32_16x16x64_bf16 v[36:39], v[24:27], v[200:207], v0\n"
        "ds_read_b128 v[76:79], v8 offset:3072\n"
        "s_waitcnt lgkmcnt(4)\n"
        "v_smfmac_f32_16x16x64_bf16 v[32:35], v[28:31], v[208:215], v0\n"
        "ds_read_b128 v[80:83], v8 offset:16384\n"
        "v_mov_b32 v56, 0\n"
        "v_smfmac_f32_16x16x64_bf16 v[36:39], v[28:31], v[216:223], v0\n"
        "ds_read_b128 v[84:87], v8 offset:17408\n"
        "s_nop 0\n"
        "v_mov_b32 v57, 0\n"
        "v_smfmac_f32_16x16x64_bf16 v[40:43], v[24:27], v[224:231], v0\n"
        "ds_read_b128 v[88:91], v8 offset:18432\n"
        "s_nop 0\n"
        "v_add_f32 v32, v32, v33\n"
        "v_add_f32 v34, v34, v35\n"
        "v_add_f32 v36, v36, v37\n"
        "v_add_f32 v38, v38, v39\n"
        "v_cvt_pk_bf16_f32 v32, v32, v36\n"
        "v_cvt_pk_bf16_f32 v33, v34, v38\n"
        "v_smfmac_f32_16x16x64_bf16 v[44:47], v[24:27], v[232:239], v0\n"
        "global_store_dwordx2 v5, v[32:33], s[44:45] sc1\n"
        "v_smfmac_f32_16x16x64_bf16 v[40:43], v[28:31], v[240:247], v0\n"
        "ds_read_b128 v[92:95], v8 offset:19456\n"
        "ds_read_b128 v[96:99], v8 offset:32768\n"
        "v_smfmac_f32_16x16x64_bf16 v[44:47], v[28:31], v[248:255], v0\n"
        "ds_read_b128 v[100:103], v8 offset:33792\n"
        "s_waitcnt lgkmcnt(6)\n"
        "v_mov_b32 v58, 0\n"
        "v_smfmac_f32_16x16x64_bf16 v[48:51], v[24:27], v[64:71], v0\n"
        "ds_read_b128 v[104:107], v8 offset:34816\n"
        "s_nop 0\n"
        "v_add_f32 v40, v40, v41\n"
        "v_add_f32 v42, v42, v43\n"
        "v_add_f32 v44, v44, v45\n"
        "v_add_f32 v46, v46, v47\n"
        "v_cvt_pk_bf16_f32 v40, v40, v44\n"
        "v_cvt_pk_bf16_f32 v41, v42, v46\n"
        "v_smfmac_f32_16x16x64_bf16 v[52:55], v[24:27], v[72:79], v0\n"
        "global_store_dwordx2 v5, v[40:41], s[46:47] sc1\n"
        "s_waitcnt lgkmcnt(3)\n"
        "v_smfmac_f32_16x16x64_bf16 v[48:51], v[28:31], v[80:87], v0\n"
        "ds_read_b128 v[108:111], v8 offset:35840\n"
        "ds_read_b128 v[112:115], v8 offset:49152\n"
        "v_mov_b32 v59, 0\n"
        "v_smfmac_f32_16x16x64_bf16 v[52:55], v[28:31], v[88:95], v0\n"
        "ds_read_b128 v[116:119], v8 offset:50176\n"
        "s_waitcnt lgkmcnt(4)\n"
        "s_nop 0\n"
        "v_smfmac_f32_16x16x64_bf16 v[56:59], v[24:27], v[96:103], v0\n"
        "ds_read_b128 v[120:123], v8 offset:51200\n"
        "ds_read_b128 v[124:127], v8 offset:52224\n"
        "s_waitcnt lgkmcnt(4)\n"
        "v_add_f32 v48, v48, v49\n"
        "v_add_f32 v50, v50, v51\n"
        "v_add_f32 v52, v52, v53\n"
        "v_add_f32 v54, v54, v55\n"
        "v_cvt_pk_bf16_f32 v48, v48, v52\n"
        "v_cvt_pk_bf16_f32 v49, v50, v54\n"
        "v_smfmac_f32_16x16x64_bf16 v[60:63], v[24:27], v[104:111], v0\n"
        "global_store_dwordx2 v5, v[48:49], s[48:49] sc1\n"
        "s_waitcnt lgkmcnt(0)\n"
        "v_smfmac_f32_16x16x64_bf16 v[56:59], v[28:31], v[112:119], v0\n"
        "v_smfmac_f32_16x16x64_bf16 v[60:63], v[28:31], v[120:127], v0\n"
        "s_nop 6\n"
        "v_add_f32 v56, v56, v57\n"
        "v_add_f32 v58, v58, v59\n"
        "v_add_f32 v60, v60, v61\n"
        "v_add_f32 v62, v62, v63\n"
        "v_cvt_pk_bf16_f32 v56, v56, v60\n"
        "v_cvt_pk_bf16_f32 v57, v58, v62\n"
        "global_store_dwordx2 v5, v[56:57], s[50:51] sc1\n"
        "ds_read_b128 v[64:67], v6 offset:0\n"
        "ds_read_b128 v[68:71], v6 offset:1024\n"
        "ds_read_b128 v[72:75], v6 offset:2048\n"
        "ds_read_b128 v[76:79], v6 offset:3072\n"
        "ds_read_b128 v[80:83], v6 offset:16384\n"
        "ds_read_b128 v[84:87], v6 offset:17408\n"
        "ds_read_b128 v[88:91], v6 offset:18432\n"
        "ds_read_b128 v[92:95], v6 offset:19456\n"
        "ds_read_b128 v[96:99], v6 offset:32768\n"
        "ds_read_b128 v[100:103], v6 offset:33792\n"
        "ds_read_b128 v[104:107], v6 offset:34816\n"
        "ds_read_b128 v[108:111], v6 offset:35840\n"
        "ds_read_b128 v[112:115], v6 offset:49152\n"
        "ds_read_b128 v[116:119], v6 offset:50176\n"
        "ds_read_b128 v[120:123], v6 offset:51200\n"
        "ds_read_b128 v[124:127], v6 offset:52224\n"
        "v_mov_b32 v60, v12\n"
        "v_mov_b32 v61, v13\n"
        "v_mov_b32 v62, v14\n"
        "v_mov_b32 v63, v15\n"
        "v_mov_b32 v8, 0\n"
        "v_mov_b32 v9, 0\n"
        "v_mov_b32 v10, 0\n"
        "v_mov_b32 v11, 0\n"
        "v_mov_b32 v12, 0\n"
        "v_mov_b32 v13, 0\n"
        "v_mov_b32 v14, 0\n"
        "v_mov_b32 v15, 0\n"
        "v_mov_b32 v16, 0\n"
        "v_mov_b32 v17, 0\n"
        "v_mov_b32 v18, 0\n"
        "v_mov_b32 v19, 0\n"
        "v_mov_b32 v20, 0\n"
        "v_mov_b32 v21, 0\n"
        "v_mov_b32 v22, 0\n"
        "v_mov_b32 v23, 0\n"
        "v_mov_b32 v24, 0\n"
        "v_mov_b32 v25, 0\n"
        "v_mov_b32 v26, 0\n"
        "v_mov_b32 v27, 0\n"
        "v_mov_b32 v28, 0\n"
        "v_mov_b32 v29, 0\n"
        "v_mov_b32 v30, 0\n"
        "v_mov_b32 v31, 0\n"
        "s_mov_b32 m0, s37\n"
        "s_nop 0\n"
        "global_load_lds_dwordx4 v1, s[38:39] sc1\n"
        "global_load_lds_dwordx4 v1, s[38:39] offset:1024 sc1\n"
        "s_mov_b32 m0, s55\n"
        "s_nop 0\n"
        "global_load_lds_dwordx4 v1, s[40:41] sc1\n"
        "global_load_lds_dwordx4 v1, s[40:41] offset:1024 sc1\n"
        "s_xor_b32 s44, s44, 0x100000\n"
        "s_xor_b32 s46, s46, 0x100000\n"
        "s_xor_b32 s48, s48, 0x100000\n"
        "s_xor_b32 s50, s50, 0x100000\n"
        "s_add_u32 s38, s38, 0x40000\n"
        "s_addc_u32 s39, s39, 0\n"
        "s_add_u32 s40, s40, 0x40000\n"
        "s_addc_u32 s41, s41, 0\n"
        "s_mov_b64 s[96:97], s[52:53]\n"
        "s_add_u32 s54, s54, 1\n"
        "s_cmp_eq_u32 s54, 3\n"
        "s_cselect_b32 s54, 0, s54\n"
        "s_cselect_b32 s65, 0xfffe0000, s98\n"
        "s_add_u32 s52, s52, s65\n"
        "s_mov_b32 m0, 2048\n"
        "s_add_u32 s36, s36, 1\n"
        "s_waitcnt vmcnt(0) lgkmcnt(0)\n"
        "s_barrier\n"
        "s_nop 15\n"
        "s_nop 10\n"
        "global_load_lds_dwordx4 v1, s[96:97] sc1\n"
        "global_load_lds_dwordx4 v1, s[96:97] offset:1024 sc1\n"
        "s_mov_b64 s[82:83], s[42:43]\n"
        "s_add_u32 s42, s42, 0x20000\n"
        "s_addc_u32 s43, s43, 0\n"
        "s_branch Lw1step%=\n"
        "Lw2step%=:\n"
        "s_barrier\n"
        "ds_read_b128 v[32:35], v1 offset:0\n"
        "ds_read_b128 v[36:39], v1 offset:1024\n"
        "s_waitcnt lgkmcnt(1)\n"
        "v_smfmac_f32_16x16x64_bf16 v[8:11], v[32:35], v[64:71], v0\n"
        "v_smfmac_f32_16x16x64_bf16 v[12:15], v[32:35], v[72:79], v0\n"
        "s_waitcnt lgkmcnt(0)\n"
        "v_smfmac_f32_16x16x64_bf16 v[8:11], v[36:39], v[80:87], v0\n"
        "v_smfmac_f32_16x16x64_bf16 v[12:15], v[36:39], v[88:95], v0\n"
        "s_barrier\n"
        "v_smfmac_f32_16x16x64_bf16 v[16:19], v[32:35], a[0:7], v0\n"
        "ds_read_b128 v[40:43], v1 offset:2048\n"
        "v_smfmac_f32_16x16x64_bf16 v[20:23], v[32:35], a[8:15], v0\n"
        "ds_read_b128 v[44:47], v1 offset:3072\n"
        "v_smfmac_f32_16x16x64_bf16 v[16:19], v[36:39], a[16:23], v0\n"
        "v_smfmac_f32_16x16x64_bf16 v[20:23], v[36:39], a[24:31], v0\n"
        "s_waitcnt lgkmcnt(1)\n"
        "v_smfmac_f32_16x16x64_bf16 v[8:11], v[40:43], v[96:103], v0\n"
        "v_smfmac_f32_16x16x64_bf16 v[12:15], v[40:43], v[104:111], v0\n"
        "s_waitcnt lgkmcnt(0)\n"
        "v_smfmac_f32_16x16x64_bf16 v[8:11], v[44:47], v[112:119], v0\n"
        "v_smfmac_f32_16x16x64_bf16 v[12:15], v[44:47], v[120:127], v0\n"
        "s_waitcnt vmcnt(0)\n"
        "s_barrier\n"
        "v_smfmac_f32_16x16x64_bf16 v[16:19], v[40:43], a[32:39], v0\n"
        "ds_read_b128 v[48:51], v1 offset:4096\n"
        "v_smfmac_f32_16x16x64_bf16 v[20:23], v[40:43], a[40:47], v0\n"
        "ds_read_b128 v[52:55], v1 offset:5120\n"
        "v_smfmac_f32_16x16x64_bf16 v[16:19], v[44:47], a[48:55], v0\n"
        "v_add_u32 v7, s55, v1\n"
        "v_smfmac_f32_16x16x64_bf16 v[20:23], v[44:47], a[56:63], v0\n"
        "ds_read_b64 v[96:97], v7 offset:0\n"
        "s_waitcnt lgkmcnt(2)\n"
        "v_smfmac_f32_16x16x64_bf16 v[8:11], v[48:51], v[128:135], v0\n"
        "ds_read_b64 v[100:101], v7 offset:8\n"
        "v_smfmac_f32_16x16x64_bf16 v[12:15], v[48:51], v[136:143], v0\n"
        "global_store_dwordx4 v1, v[60:63], s[82:83] sc1\n"
        "ds_read_b64 v[104:105], v7 offset:1024\n"
        "s_waitcnt lgkmcnt(3)\n"
        "v_smfmac_f32_16x16x64_bf16 v[8:11], v[52:55], v[144:151], v0\n"
        "ds_read_b64 v[108:109], v7 offset:1032\n"
        "v_smfmac_f32_16x16x64_bf16 v[12:15], v[52:55], v[152:159], v0\n"
        "s_barrier\n"
        "v_smfmac_f32_16x16x64_bf16 v[16:19], v[48:51], a[64:71], v0\n"
        "ds_read_b128 v[56:59], v1 offset:6144\n"
        "v_smfmac_f32_16x16x64_bf16 v[20:23], v[48:51], a[72:79], v0\n"
        "ds_read_b128 v[60:63], v1 offset:7168\n"
        "v_smfmac_f32_16x16x64_bf16 v[16:19], v[52:55], a[80:87], v0\n"
        "ds_read_b128 v[64:67], v1 offset:8192\n"
        "v_smfmac_f32_16x16x64_bf16 v[20:23], v[52:55], a[88:95], v0\n"
        "ds_read_b128 v[68:71], v1 offset:12288\n"
        "s_waitcnt lgkmcnt(3)\n"
        "v_smfmac_f32_16x16x64_bf16 v[8:11], v[56:59], v[160:167], v0\n"
        "ds_read_b128 v[72:75], v1 offset:9216\n"
        "v_smfmac_f32_16x16x64_bf16 v[12:15], v[56:59], v[168:175], v0\n"
        "ds_read_b128 v[76:79], v1 offset:13312\n"
        "s_waitcnt lgkmcnt(4)\n"
        "v_smfmac_f32_16x16x64_bf16 v[8:11], v[60:63], v[176:183], v0\n"
        "ds_read_b128 v[80:83], v1 offset:10240\n"
        "v_smfmac_f32_16x16x64_bf16 v[12:15], v[60:63], v[184:191], v0\n"
        "ds_read_b128 v[84:87], v1 offset:14336\n"
        "v_smfmac_f32_16x16x64_bf16 v[16:19], v[56:59], a[96:103], v0\n"
        "ds_read_b128 v[88:91], v1 offset:11264\n"
        "s_nop 0\n"
        "v_smfmac_f32_16x16x64_bf16 v[20:23], v[56:59], a[104:111], v0\n"
        "ds_read_b128 v[92:95], v1 offset:15360\n"
        "s_nop 0\n"
        "v_smfmac_f32_16x16x64_bf16 v[16:19], v[60:63], a[112:119], v0\n"
        "v_add_f32 v8, v8, v9\n"
        "v_smfmac_f32_16x16x64_bf16 v[20:23], v[60:63], a[120:127], v0\n"
        "v_add_f32 v12, v12, v13\n"
        "v_add_f32 v10, v10, v11\n"
        "v_add_f32 v14, v14, v15\n"
        "v_cvt_pk_bf16_f32 v9, v8, v12\n"
        "v_cvt_pk_bf16_f32 v11, v10, v14\n"
        "ds_write2_b32 v2, v9, v11 offset1:8\n"
        "v_cmp_u_f32 vcc, v8, v10\n"
        "v_add_f32 v16, v16, v17\n"
        "v_add_f32 v20, v20, v21\n"
        "v_add_f32 v18, v18, v19\n"
        "v_add_f32 v22, v22, v23\n"
        "v_cvt_pk_bf16_f32 v99, v16, v20\n"
        "v_cvt_pk_bf16_f32 v103, v18, v22\n"
        "v_smfmac_f32_16x16x64_bf16 v[24:27], v[32:35], a[128:135], v0\n"
        "ds_write2_b32 v2, v99, v103 offset0:128 offset1:136\n"
        "v_smfmac_f32_16x16x64_bf16 v[28:31], v[32:35], a[136:143], v0\n"
        "s_cbranch_vccnz Lw2slow%=\n"
        "Lw2back%=:\n"
        "s_cmp_eq_u32 s36, 63\n"
        "s_cbranch_scc1 Llast%=\n"
        "v_smfmac_f32_16x16x64_bf16 v[24:27], v[36:39], a[144:151], v0\n"
        "v_mov_b32 v16, 0\n"
        "v_smfmac_f32_16x16x64_bf16 v[28:31], v[36:39], a[152:159], v0\n"
        "v_mov_b32 v17, 0\n"
        "v_smfmac_f32_16x16x64_bf16 v[24:27], v[40:43], a[160:167], v0\n"
        "s_waitcnt lgkmcnt(0)\n"
        "s_barrier\n"
        "ds_read_b128 v[112:115], v3 offset:24576\n"
        "v_smfmac_f32_16x16x64_bf16 v[28:31], v[40:43], a[168:175], v0\n"
        "ds_read_b128 v[116:119], v3 offset:25600\n"
        "v_mov_b32 v18, 0\n"
        "v_smfmac_f32_16x16x64_bf16 v[24:27], v[44:47], a[176:183], v0\n"
        "ds_read_b128 v[120:123], v3 offset:26624\n"
        "v_mov_b32 v19, 0\n"
        "v_smfmac_f32_16x16x64_bf16 v[28:31], v[44:47], a[184:191], v0\n"
        "ds_read_b128 v[124:127], v3 offset:27648\n"
        "v_mov_b32 v20, 0\n"
        "v_smfmac_f32_16x16x64_bf16 v[24:27], v[48:51], a[192:199], v0\n"
        "v_mov_b32 v21, 0\n"
        "s_waitcnt lgkmcnt(3)\n"
        "v_mfma_f32_16x16x32_bf16 v[8:11], v[64:67], v[112:115], 0\n"
        "ds_read_b128 v[64:67], v4 offset:25088\n"
        "v_mov_b32 v22, 0\n"
        "v_mfma_f32_16x16x32_bf16 v[12:15], v[68:71], v[112:115], 0\n"
        "ds_read_b128 v[68:71], v4 offset:26112\n"
        "v_mov_b32 v23, 0\n"
        "s_waitcnt lgkmcnt(4)\n"
        "v_mfma_f32_16x16x32_bf16 v[8:11], v[72:75], v[116:119], v[8:11]\n"
        "ds_read_b128 v[72:75], v4 offset:27136\n"
        "v_mov_b32 v32, 0x3f803f80\n"
        "v_mfma_f32_16x16x32_bf16 v[12:15], v[76:79], v[116:119], v[12:15]\n"
        "ds_read_b128 v[76:79], v4 offset:28160\n"
        "v_mov_b32 v33, 0x3f00\n"
        "s_waitcnt lgkmcnt(5)\n"
        "v_mfma_f32_16x16x32_bf16 v[8:11], v[80:83], v[120:123], v[8:11]\n"
        "v_mov_b32 v34, 0x3f803f80\n"
        "v_mfma_f32_16x16x32_bf16 v[12:15], v[84:87], v[120:123], v[12:15]\n"
        "v_mov_b32 v35, 0x3f00\n"
        "s_waitcnt lgkmcnt(4)\n"
        "v_mfma_f32_16x16x32_bf16 v[8:11], v[88:91], v[124:127], v[8:11]\n"
        "v_mfma_f32_16x16x32_bf16 v[12:15], v[92:95], v[124:127], v[12:15]\n"
        "s_waitcnt lgkmcnt(0)\n"
        "s_nop 1\n"
        "v_mfma_f32_16x16x32_bf16 v[80:83], v[64:67], v[112:115], 0\n"
        "s_nop 2\n"
        "v_mfma_f32_16x16x32_bf16 v[80:83], v[68:71], v[116:119], v[80:83]\n"
        "v_cndmask_b32 v8, v8, v12, s[58:59]\n"
        "v_cndmask_b32 v9, v9, v13, s[58:59]\n"
        "v_mfma_f32_16x16x32_bf16 v[80:83], v[72:75], v[120:123], v[80:83]\n"
        "v_cndmask_b32 v10, v10, v14, s[58:59]\n"
        "v_cndmask_b32 v11, v11, v15, s[58:59]\n"
        "v_mfma_f32_16x16x32_bf16 v[80:83], v[76:79], v[124:127], v[80:83]\n"
        "v_max3_f32 v12, v8, v9, v10\n"
        "v_max_f32 v12, v12, v11\n"
        "v_smfmac_f32_16x16x64_bf16 v[28:31], v[48:51], a[200:207], v0\n"
        "s_nop 0\n"
        "v_max_f32_dpp v12, v12, v12 quad_perm:[1,0,3,2] row_mask:0xf bank_mask:0xf\n"
        "v_smfmac_f32_16x16x64_bf16 v[24:27], v[52:55], a[208:215], v0\n"
        "v_mov_b32 v13, v12\n"
        "v_smfmac_f32_16x16x64_bf16 v[28:31], v[52:55], a[216:223], v0\n"
        "v_cndmask_b32 v14, v80, v81, s[58:59]\n"
        "v_permlane32_swap_b32 v12, v13\n"
        "v_max3_f32 v12, v12, v13, v80\n"
        "v_smfmac_f32_16x16x64_bf16 v[24:27], v[56:59], a[224:231], v0\n"
        "v_max3_f32 v12, v12, v81, v82\n"
        "v_max_f32 v13, v12, v83\n"
        "v_max_f32 v12, v12, v83\n"
        "v_smfmac_f32_16x16x64_bf16 v[28:31], v[56:59], a[232:239], v0\n"
        "v_cndmask_b32 v15, v82, v83, s[58:59]\n"
        "v_permlane16_swap_b32 v12, v13\n"
        "v_smfmac_f32_16x16x64_bf16 v[24:27], v[60:63], a[240:247], v0\n"
        "v_max_f32 v12, v12, v13\n"
        "v_smfmac_f32_16x16x64_bf16 v[28:31], v[60:63], a[248:255], v0\n"
        "v_mul_f32 v12, s60, v12\n"
        "v_fma_f32 v8, v8, s60, -v12\n"
        "v_fma_f32 v9, v9, s60, -v12\n"
        "v_fma_f32 v10, v10, s60, -v12\n"
        "v_fma_f32 v11, v11, s60, -v12\n"
        "v_fma_f32 v14, v14, s60, -v12\n"
        "v_fma_f32 v15, v15, s60, -v12\n"
        "v_exp_f32 v8, v8\n"
        "v_exp_f32 v9, v9\n"
        "v_exp_f32 v10, v10\n"
        "v_exp_f32 v11, v11\n"
        "v_exp_f32 v14, v14\n"
        "v_exp_f32 v15, v15\n"
        "v_cvt_pk_bf16_f32 v36, v8, v9\n"
        "v_cvt_pk_bf16_f32 v38, v10, v11\n"
        "v_cvt_pk_bf16_f32 v37, v14, 0\n"
        "v_cvt_pk_bf16_f32 v39, v15, 0\n"
        "v_cvt_pk_bf16_f32 v98, v24, v26\n"
        "v_cvt_pk_bf16_f32 v102, v25, v27\n"
        "v_mfma_f32_16x16x32_bf16 v[84:87], v[36:39], v[32:35], 0\n"
        "v_cvt_pk_bf16_f32 v106, v28, v30\n"
        "v_cvt_pk_bf16_f32 v110, v29, v31\n"
        "v_smfmac_f32_16x16x64_bf16 v[16:19], v[36:39], v[96:103], v0 abid:1\n"
        "v_add_u32 v9, 3072, v2\n"
        "v_smfmac_f32_16x16x64_bf16 v[20:23], v[36:39], v[104:111], v0 abid:1\n"
        "s_nop 1\n"
        "v_mov_b32 v61, 0\n"
        "v_add_f32 v84, v84, v85\n"
        "v_add_f32 v86, v86, v87\n"
        "v_rcp_f32 v84, v84\n"
        "v_rcp_f32 v86, v86\n"
        "v_add_f32 v16, v16, v17\n"
        "v_add_f32 v20, v20, v21\n"
        "v_add_f32 v18, v18, v19\n"
        "v_mul_f32 v16, v16, v84\n"
        "v_mul_f32 v20, v20, v84\n"
        "v_add_f32 v22, v22, v23\n"
        "v_cvt_pk_bf16_f32 v17, v16, v20\n"
        "v_mul_f32 v18, v18, v86\n"
        "v_mul_f32 v22, v22, v86\n"
        "v_cvt_pk_bf16_f32 v19, v18, v22\n"
        "ds_write2_b32 v9, v17, v19 offset1:8\n"
        "v_mov_b32 v60, 0\n"
        "v_mov_b32 v62, 0\n"
        "v_mov_b32 v63, 0\n"
        "v_add_f32 v10, v24, v25\n"
        "v_add_f32 v11, v26, v27\n"
        "v_mfma_f32_16x16x32_bf16 v[32:35], v[60:63], v[60:63], 0\n"
        "v_add_f32 v16, v28, v29\n"
        "v_add_f32 v18, v30, v31\n"
        "v_mfma_f32_16x16x32_bf16 v[36:39], v[60:63], v[60:63], 0\n"
        "v_mfma_f32_16x16x32_bf16 v[40:43], v[60:63], v[60:63], 0\n"
        "s_waitcnt lgkmcnt(0)\n"
        "s_barrier\n"
        "ds_read_b128 v[24:27], v1 offset:28672\n"
        "v_mfma_f32_16x16x32_bf16 v[44:47], v[60:63], v[60:63], 0\n"
        "ds_read_b128 v[28:31], v1 offset:29696\n"
        "v_add_u32 v8, 0x10000, v6\n"
        "v_mov_b32 v12, v99\n"
        "v_mfma_f32_16x16x32_bf16 v[48:51], v[60:63], v[60:63], 0\n"
        "ds_read_b128 v[64:67], v8 offset:0\n"
        "v_mov_b32 v13, v103\n"
        "v_cvt_pk_bf16_f32 v14, v10, v11\n"
        "v_mfma_f32_16x16x32_bf16 v[52:55], v[60:63], v[60:63], 0\n"
        "ds_read_b128 v[68:71], v8 offset:1024\n"
        "v_cvt_pk_bf16_f32 v15, v16, v18\n"
        "s_waitcnt lgkmcnt(3)\n"
        "v_smfmac_f32_16x16x64_bf16 v[32:35], v[24:27], v[192:199], v0\n"
        "ds_read_b128 v[72:75], v8 offset:2048\n"
        "v_smfmac_f32_16x16x64_bf16 v[36:39], v[24:27], v[200:207], v0\n"
        "ds_read_b128 v[76:79], v8 offset:3072\n"
        "s_waitcnt lgkmcnt(4)\n"
        "v_smfmac_f32_16x16x64_bf16 v[32:35], v[28:31], v[208:215], v0\n"
        "ds_read_b128 v[80:83], v8 offset:16384\n"
        "v_mov_b32 v56, 0\n"
        "v_smfmac_f32_16x16x64_bf16 v[36:39], v[28:31], v[216:223], v0\n"
        "ds_read_b128 v[84:87], v8 offset:17408\n"
        "s_nop 0\n"
        "v_mov_b32 v57, 0\n"
        "v_smfmac_f32_16x16x64_bf16 v[40:43], v[24:27], v[224:231], v0\n"
        "ds_read_b128 v[88:91], v8 offset:18432\n"
        "s_nop 0\n"
        "v_add_f32 v32, v32, v33\n"
        "v_add_f32 v34, v34, v35\n"
        "v_add_f32 v36, v36, v37\n"
        "v_add_f32 v38, v38, v39\n"
        "v_cvt_pk_bf16_f32 v32, v32, v36\n"
        "v_cvt_pk_bf16_f32 v33, v34, v38\n"
        "v_smfmac_f32_16x16x64_bf16 v[44:47], v[24:27], v[232:239], v0\n"
        "global_store_dwordx2 v5, v[32:33], s[44:45] sc1\n"
        "v_smfmac_f32_16x16x64_bf16 v[40:43], v[28:31], v[240:247], v0\n"
        "ds_read_b128 v[92:95], v8 offset:19456\n"
        "ds_read_b128 v[96:99], v8 offset:32768\n"
        "v_smfmac_f32_16x16x64_bf16 v[44:47], v[28:31], v[248:255], v0\n"
        "ds_read_b128 v[100:103], v8 offset:33792\n"
        "s_waitcnt lgkmcnt(6)\n"
        "v_mov_b32 v58, 0\n"
        "v_smfmac_f32_16x16x64_bf16 v[48:51], v[24:27], v[64:71], v0\n"
        "ds_read_b128 v[104:107], v8 offset:34816\n"
        "s_nop 0\n"
        "v_add_f32 v40, v40, v41\n"
        "v_add_f32 v42, v42, v43\n"
        "v_add_f32 v44, v44, v45\n"
        "v_add_f32 v46, v46, v47\n"
        "v_cvt_pk_bf16_f32 v40, v40, v44\n"
        "v_cvt_pk_bf16_f32 v41, v42, v46\n"
        "v_smfmac_f32_16x16x64_bf16 v[52:55], v[24:27], v[72:79], v0\n"
        "global_store_dwordx2 v5, v[40:41], s[46:47] sc1\n"
        "s_waitcnt lgkmcnt(3)\n"
        "v_smfmac_f32_16x16x64_bf16 v[48:51], v[28:31], v[80:87], v0\n"
        "ds_read_b128 v[108:111], v8 offset:35840\n"
        "ds_read_b128 v[112:115], v8 offset:49152\n"
        "v_mov_b32 v59, 0\n"
        "v_smfmac_f32_16x16x64_bf16 v[52:55], v[28:31], v[88:95], v0\n"
        "ds_read_b128 v[116:119], v8 offset:50176\n"
        "s_waitcnt lgkmcnt(4)\n"
        "s_nop 0\n"
        "v_smfmac_f32_16x16x64_bf16 v[56:59], v[24:27], v[96:103], v0\n"
        "ds_read_b128 v[120:123], v8 offset:51200\n"
        "ds_read_b128 v[124:127], v8 offset:52224\n"
        "s_waitcnt lgkmcnt(4)\n"
        "v_add_f32 v48, v48, v49\n"
        "v_add_f32 v50, v50, v51\n"
        "v_add_f32 v52, v52, v53\n"
        "v_add_f32 v54, v54, v55\n"
        "v_cvt_pk_bf16_f32 v48, v48, v52\n"
        "v_cvt_pk_bf16_f32 v49, v50, v54\n"
        "v_smfmac_f32_16x16x64_bf16 v[60:63], v[24:27], v[104:111], v0\n"
        "global_store_dwordx2 v5, v[48:49], s[48:49] sc1\n"
        "s_waitcnt lgkmcnt(0)\n"
        "v_smfmac_f32_16x16x64_bf16 v[56:59], v[28:31], v[112:119], v0\n"
        "v_smfmac_f32_16x16x64_bf16 v[60:63], v[28:31], v[120:127], v0\n"
        "s_nop 6\n"
        "v_add_f32 v56, v56, v57\n"
        "v_add_f32 v58, v58, v59\n"
        "v_add_f32 v60, v60, v61\n"
        "v_add_f32 v62, v62, v63\n"
        "v_cvt_pk_bf16_f32 v56, v56, v60\n"
        "v_cvt_pk_bf16_f32 v57, v58, v62\n"
        "global_store_dwordx2 v5, v[56:57], s[50:51] sc1\n"
        "ds_read_b128 v[64:67], v6 offset:0\n"
        "ds_read_b128 v[68:71], v6 offset:1024\n"
        "ds_read_b128 v[72:75], v6 offset:2048\n"
        "ds_read_b128 v[76:79], v6 offset:3072\n"
        "ds_read_b128 v[80:83], v6 offset:16384\n"
        "ds_read_b128 v[84:87], v6 offset:17408\n"
        "ds_read_b128 v[88:91], v6 offset:18432\n"
        "ds_read_b128 v[92:95], v6 offset:19456\n"
        "ds_read_b128 v[96:99], v6 offset:32768\n"
        "ds_read_b128 v[100:103], v6 offset:33792\n"
        "ds_read_b128 v[104:107], v6 offset:34816\n"
        "ds_read_b128 v[108:111], v6 offset:35840\n"
        "ds_read_b128 v[112:115], v6 offset:49152\n"
        "ds_read_b128 v[116:119], v6 offset:50176\n"
        "ds_read_b128 v[120:123], v6 offset:51200\n"
        "ds_read_b128 v[124:127], v6 offset:52224\n"
        "v_mov_b32 v60, v12\n"
        "v_mov_b32 v61, v13\n"
        "v_mov_b32 v62, v14\n"
        "v_mov_b32 v63, v15\n"
        "v_mov_b32 v8, 0\n"
        "v_mov_b32 v9, 0\n"
        "v_mov_b32 v10, 0\n"
        "v_mov_b32 v11, 0\n"
        "v_mov_b32 v12, 0\n"
        "v_mov_b32 v13, 0\n"
        "v_mov_b32 v14, 0\n"
        "v_mov_b32 v15, 0\n"
        "v_mov_b32 v16, 0\n"
        "v_mov_b32 v17, 0\n"
        "v_mov_b32 v18, 0\n"
        "v_mov_b32 v19, 0\n"
        "v_mov_b32 v20, 0\n"
        "v_mov_b32 v21, 0\n"
        "v_mov_b32 v22, 0\n"
        "v_mov_b32 v23, 0\n"
        "v_mov_b32 v24, 0\n"
        "v_mov_b32 v25, 0\n"
        "v_mov_b32 v26, 0\n"
        "v_mov_b32 v27, 0\n"
        "v_mov_b32 v28, 0\n"
        "v_mov_b32 v29, 0\n"
        "v_mov_b32 v30, 0\n"
        "v_mov_b32 v31, 0\n"
        "s_mov_b32 m0, s37\n"
        "s_nop 0\n"
        "global_load_lds_dwordx4 v1, s[38:39] sc1\n"
        "global_load_lds_dwordx4 v1, s[38:39] offset:1024 sc1\n"
        "s_mov_b32 m0, s55\n"
        "s_nop 0\n"
        "global_load_lds_dwordx4 v1, s[40:41] sc1\n"
        "global_load_lds_dwordx4 v1, s[40:41] offset:1024 sc1\n"
        "s_xor_b32 s44, s44, 0x100000\n"
        "s_xor_b32 s46, s46, 0x100000\n"
        "s_xor_b32 s48, s48, 0x100000\n"
        "s_xor_b32 s50, s50, 0x100000\n"
        "s_add_u32 s38, s38, 0x40000\n"
        "s_addc_u32 s39, s39, 0\n"
        "s_add_u32 s40, s40, 0x40000\n"
        "s_addc_u32 s41, s41, 0\n"
        "s_mov_b64 s[96:97], s[52:53]\n"
        "s_add_u32 s54, s54, 1\n"
        "s_cmp_eq_u32 s54, 3\n"
        "s_cselect_b32 s54, 0, s54\n"
        "s_cselect_b32 s65, 0xfffe0000, s98\n"
        "s_add_u32 s52, s52, s65\n"
        "s_mov_b32 m0, 4096\n"
        "s_add_u32 s36, s36, 1\n"
        "s_waitcnt vmcnt(0) lgkmcnt(0)\n"
        "s_barrier\n"
        "s_nop 15\n"
        "s_nop 15\n"
        "s_nop 15\n"
        "s_nop 5\n"
        "global_load_lds_dwordx4 v1, s[96:97] sc1\n"
        "global_load_lds_dwordx4 v1, s[96:97] offset:1024 sc1\n"
        "s_mov_b64 s[82:83], s[42:43]\n"
        "s_add_u32 s42, s42, 0x20000\n"
        "s_addc_u32 s43, s43, 0\n"
        "s_branch Lw2step%=\n"
        "Lw3step%=:\n"
        "s_barrier\n"
        "ds_read_b128 v[32:35], v1 offset:0\n"
        "ds_read_b128 v[36:39], v1 offset:1024\n"
        "s_waitcnt lgkmcnt(1)\n"
        "v_smfmac_f32_16x16x64_bf16 v[8:11], v[32:35], v[64:71], v0\n"
        "v_smfmac_f32_16x16x64_bf16 v[12:15], v[32:35], v[72:79], v0\n"
        "s_waitcnt lgkmcnt(0)\n"
        "v_smfmac_f32_16x16x64_bf16 v[8:11], v[36:39], v[80:87], v0\n"
        "v_smfmac_f32_16x16x64_bf16 v[12:15], v[36:39], v[88:95], v0\n"
        "s_barrier\n"
        "v_smfmac_f32_16x16x64_bf16 v[16:19], v[32:35], a[0:7], v0\n"
        "ds_read_b128 v[40:43], v1 offset:2048\n"
        "v_smfmac_f32_16x16x64_bf16 v[20:23], v[32:35], a[8:15], v0\n"
        "ds_read_b128 v[44:47], v1 offset:3072\n"
        "v_smfmac_f32_16x16x64_bf16 v[16:19], v[36:39], a[16:23], v0\n"
        "v_smfmac_f32_16x16x64_bf16 v[20:23], v[36:39], a[24:31], v0\n"
        "s_waitcnt lgkmcnt(1)\n"
        "v_smfmac_f32_16x16x64_bf16 v[8:11], v[40:43], v[96:103], v0\n"
        "v_smfmac_f32_16x16x64_bf16 v[12:15], v[40:43], v[104:111], v0\n"
        "s_waitcnt lgkmcnt(0)\n"
        "v_smfmac_f32_16x16x64_bf16 v[8:11], v[44:47], v[112:119], v0\n"
        "v_smfmac_f32_16x16x64_bf16 v[12:15], v[44:47], v[120:127], v0\n"
        "s_barrier\n"
        "v_smfmac_f32_16x16x64_bf16 v[16:19], v[40:43], a[32:39], v0\n"
        "ds_read_b128 v[48:51], v1 offset:4096\n"
        "v_smfmac_f32_16x16x64_bf16 v[20:23], v[40:43], a[40:47], v0\n"
        "ds_read_b128 v[52:55], v1 offset:5120\n"
        "v_smfmac_f32_16x16x64_bf16 v[16:19], v[44:47], a[48:55], v0\n"
        "v_add_u32 v7, s55, v1\n"
        "v_smfmac_f32_16x16x64_bf16 v[20:23], v[44:47], a[56:63], v0\n"
        "ds_read_b64 v[96:97], v7 offset:0\n"
        "s_waitcnt lgkmcnt(2)\n"
        "v_smfmac_f32_16x16x64_bf16 v[8:11], v[48:51], v[128:135], v0\n"
        "ds_read_b64 v[100:101], v7 offset:8\n"
        "v_smfmac_f32_16x16x64_bf16 v[12:15], v[48:51], v[136:143], v0\n"
        "ds_read_b64 v[104:105], v7 offset:1024\n"
        "s_waitcnt lgkmcnt(3)\n"
        "v_smfmac_f32_16x16x64_bf16 v[8:11], v[52:55], v[144:151], v0\n"
        "global_store_dwordx4 v1, v[60:63], s[82:83] sc1\n"
        "ds_read_b64 v[108:109], v7 offset:1032\n"
        "v_smfmac_f32_16x16x64_bf16 v[12:15], v[52:55], v[152:159], v0\n"
        "s_waitcnt vmcnt(1)\n"
        "s_barrier\n"
        "v_smfmac_f32_16x16x64_bf16 v[16:19], v[48:51], a[64:71], v0\n"
        "ds_read_b128 v[56:59], v1 offset:6144\n"
        "v_smfmac_f32_16x16x64_bf16 v[20:23], v[48:51], a[72:79], v0\n"
        "ds_read_b128 v[60:63], v1 offset:7168\n"
        "v_smfmac_f32_16x16x64_bf16 v[16:19], v[52:55], a[80:87], v0\n"
        "ds_read_b128 v[64:67], v1 offset:8192\n"
        "v_smfmac_f32_16x16x64_bf16 v[20:23], v[52:55], a[88:95], v0\n"
        "ds_read_b128 v[68:71], v1 offset:12288\n"
        "s_waitcnt lgkmcnt(3)\n"
        "v_smfmac_f32_16x16x64_bf16 v[8:11], v[56:59], v[160:167], v0\n"
        "ds_read_b128 v[72:75], v1 offset:9216\n"
        "v_smfmac_f32_16x16x64_bf16 v[12:15], v[56:59], v[168:175], v0\n"
        "ds_read_b128 v[76:79], v1 offset:13312\n"
        "s_waitcnt lgkmcnt(4)\n"
        "v_smfmac_f32_16x16x64_bf16 v[8:11], v[60:63], v[176:183], v0\n"
        "ds_read_b128 v[80:83], v1 offset:10240\n"
        "v_smfmac_f32_16x16x64_bf16 v[12:15], v[60:63], v[184:191], v0\n"
        "ds_read_b128 v[84:87], v1 offset:14336\n"
        "v_smfmac_f32_16x16x64_bf16 v[16:19], v[56:59], a[96:103], v0\n"
        "ds_read_b128 v[88:91], v1 offset:11264\n"
        "s_nop 0\n"
        "v_smfmac_f32_16x16x64_bf16 v[20:23], v[56:59], a[104:111], v0\n"
        "ds_read_b128 v[92:95], v1 offset:15360\n"
        "s_nop 0\n"
        "v_smfmac_f32_16x16x64_bf16 v[16:19], v[60:63], a[112:119], v0\n"
        "v_add_f32 v8, v8, v9\n"
        "v_smfmac_f32_16x16x64_bf16 v[20:23], v[60:63], a[120:127], v0\n"
        "v_add_f32 v12, v12, v13\n"
        "v_add_f32 v10, v10, v11\n"
        "v_add_f32 v14, v14, v15\n"
        "v_cvt_pk_bf16_f32 v9, v8, v12\n"
        "v_cvt_pk_bf16_f32 v11, v10, v14\n"
        "ds_write2_b32 v2, v9, v11 offset1:8\n"
        "v_cmp_u_f32 vcc, v8, v10\n"
        "v_add_f32 v16, v16, v17\n"
        "v_add_f32 v20, v20, v21\n"
        "v_add_f32 v18, v18, v19\n"
        "v_add_f32 v22, v22, v23\n"
        "v_cvt_pk_bf16_f32 v99, v16, v20\n"
        "v_cvt_pk_bf16_f32 v103, v18, v22\n"
        "v_smfmac_f32_16x16x64_bf16 v[24:27], v[32:35], a[128:135], v0\n"
        "ds_write2_b32 v2, v99, v103 offset0:128 offset1:136\n"
        "v_smfmac_f32_16x16x64_bf16 v[28:31], v[32:35], a[136:143], v0\n"
        "s_cbranch_vccnz Lw3slow%=\n"
        "Lw3back%=:\n"
        "s_cmp_eq_u32 s36, 63\n"
        "s_cbranch_scc1 Llast%=\n"
        "v_smfmac_f32_16x16x64_bf16 v[24:27], v[36:39], a[144:151], v0\n"
        "v_mov_b32 v16, 0\n"
        "v_smfmac_f32_16x16x64_bf16 v[28:31], v[36:39], a[152:159], v0\n"
        "v_mov_b32 v17, 0\n"
        "v_smfmac_f32_16x16x64_bf16 v[24:27], v[40:43], a[160:167], v0\n"
        "s_waitcnt lgkmcnt(0)\n"
        "s_barrier\n"
        "ds_read_b128 v[112:115], v3 offset:24576\n"
        "v_smfmac_f32_16x16x64_bf16 v[28:31], v[40:43], a[168:175], v0\n"
        "ds_read_b128 v[116:119], v3 offset:25600\n"
        "v_mov_b32 v18, 0\n"
        "v_smfmac_f32_16x16x64_bf16 v[24:27], v[44:47], a[176:183], v0\n"
        "ds_read_b128 v[120:123], v3 offset:26624\n"
        "v_mov_b32 v19, 0\n"
        "v_smfmac_f32_16x16x64_bf16 v[28:31], v[44:47], a[184:191], v0\n"
        "ds_read_b128 v[124:127], v3 offset:27648\n"
        "v_mov_b32 v20, 0\n"
        "v_smfmac_f32_16x16x64_bf16 v[24:27], v[48:51], a[192:199], v0\n"
        "v_mov_b32 v21, 0\n"
        "s_waitcnt lgkmcnt(3)\n"
        "v_mfma_f32_16x16x32_bf16 v[8:11], v[64:67], v[112:115], 0\n"
        "ds_read_b128 v[64:67], v4 offset:25088\n"
        "v_mov_b32 v22, 0\n"
        "v_mfma_f32_16x16x32_bf16 v[12:15], v[68:71], v[112:115], 0\n"
        "ds_read_b128 v[68:71], v4 offset:26112\n"
        "v_mov_b32 v23, 0\n"
        "s_waitcnt lgkmcnt(4)\n"
        "v_mfma_f32_16x16x32_bf16 v[8:11], v[72:75], v[116:119], v[8:11]\n"
        "ds_read_b128 v[72:75], v4 offset:27136\n"
        "v_mov_b32 v32, 0x3f803f80\n"
        "v_mfma_f32_16x16x32_bf16 v[12:15], v[76:79], v[116:119], v[12:15]\n"
        "ds_read_b128 v[76:79], v4 offset:28160\n"
        "v_mov_b32 v33, 0x3f00\n"
        "s_waitcnt lgkmcnt(5)\n"
        "v_mfma_f32_16x16x32_bf16 v[8:11], v[80:83], v[120:123], v[8:11]\n"
        "v_mov_b32 v34, 0x3f803f80\n"
        "v_mfma_f32_16x16x32_bf16 v[12:15], v[84:87], v[120:123], v[12:15]\n"
        "v_mov_b32 v35, 0x3f00\n"
        "s_waitcnt lgkmcnt(4)\n"
        "v_mfma_f32_16x16x32_bf16 v[8:11], v[88:91], v[124:127], v[8:11]\n"
        "v_mfma_f32_16x16x32_bf16 v[12:15], v[92:95], v[124:127], v[12:15]\n"
        "s_waitcnt lgkmcnt(0)\n"
        "s_nop 1\n"
        "v_mfma_f32_16x16x32_bf16 v[80:83], v[64:67], v[112:115], 0\n"
        "s_nop 2\n"
        "v_mfma_f32_16x16x32_bf16 v[80:83], v[68:71], v[116:119], v[80:83]\n"
        "v_cndmask_b32 v8, v8, v12, s[58:59]\n"
        "v_cndmask_b32 v9, v9, v13, s[58:59]\n"
        "v_mfma_f32_16x16x32_bf16 v[80:83], v[72:75], v[120:123], v[80:83]\n"
        "v_cndmask_b32 v10, v10, v14, s[58:59]\n"
        "v_cndmask_b32 v11, v11, v15, s[58:59]\n"
        "v_mfma_f32_16x16x32_bf16 v[80:83], v[76:79], v[124:127], v[80:83]\n"
        "v_max3_f32 v12, v8, v9, v10\n"
        "v_max_f32 v12, v12, v11\n"
        "v_smfmac_f32_16x16x64_bf16 v[28:31], v[48:51], a[200:207], v0\n"
        "s_nop 0\n"
        "v_max_f32_dpp v12, v12, v12 quad_perm:[1,0,3,2] row_mask:0xf bank_mask:0xf\n"
        "v_smfmac_f32_16x16x64_bf16 v[24:27], v[52:55], a[208:215], v0\n"
        "v_mov_b32 v13, v12\n"
        "v_smfmac_f32_16x16x64_bf16 v[28:31], v[52:55], a[216:223], v0\n"
        "v_cndmask_b32 v14, v80, v81, s[58:59]\n"
        "v_permlane32_swap_b32 v12, v13\n"
        "v_max3_f32 v12, v12, v13, v80\n"
        "v_smfmac_f32_16x16x64_bf16 v[24:27], v[56:59], a[224:231], v0\n"
        "v_max3_f32 v12, v12, v81, v82\n"
        "v_max_f32 v13, v12, v83\n"
        "v_max_f32 v12, v12, v83\n"
        "v_smfmac_f32_16x16x64_bf16 v[28:31], v[56:59], a[232:239], v0\n"
        "v_cndmask_b32 v15, v82, v83, s[58:59]\n"
        "v_permlane16_swap_b32 v12, v13\n"
        "v_smfmac_f32_16x16x64_bf16 v[24:27], v[60:63], a[240:247], v0\n"
        "v_max_f32 v12, v12, v13\n"
        "v_smfmac_f32_16x16x64_bf16 v[28:31], v[60:63], a[248:255], v0\n"
        "v_mul_f32 v12, s60, v12\n"
        "v_fma_f32 v8, v8, s60, -v12\n"
        "v_fma_f32 v9, v9, s60, -v12\n"
        "v_fma_f32 v10, v10, s60, -v12\n"
        "v_fma_f32 v11, v11, s60, -v12\n"
        "v_fma_f32 v14, v14, s60, -v12\n"
        "v_fma_f32 v15, v15, s60, -v12\n"
        "v_exp_f32 v8, v8\n"
        "v_exp_f32 v9, v9\n"
        "v_exp_f32 v10, v10\n"
        "v_exp_f32 v11, v11\n"
        "v_exp_f32 v14, v14\n"
        "v_exp_f32 v15, v15\n"
        "v_cvt_pk_bf16_f32 v36, v8, v9\n"
        "v_cvt_pk_bf16_f32 v38, v10, v11\n"
        "v_cvt_pk_bf16_f32 v37, v14, 0\n"
        "v_cvt_pk_bf16_f32 v39, v15, 0\n"
        "v_cvt_pk_bf16_f32 v98, v24, v26\n"
        "v_cvt_pk_bf16_f32 v102, v25, v27\n"
        "v_mfma_f32_16x16x32_bf16 v[84:87], v[36:39], v[32:35], 0\n"
        "v_cvt_pk_bf16_f32 v106, v28, v30\n"
        "v_cvt_pk_bf16_f32 v110, v29, v31\n"
        "v_smfmac_f32_16x16x64_bf16 v[16:19], v[36:39], v[96:103], v0 abid:1\n"
        "v_add_u32 v9, 2560, v2\n"
        "v_smfmac_f32_16x16x64_bf16 v[20:23], v[36:39], v[104:111], v0 abid:1\n"
        "s_nop 1\n"
        "v_mov_b32 v61, 0\n"
        "v_add_f32 v84, v84, v85\n"
        "v_add_f32 v86, v86, v87\n"
        "v_rcp_f32 v84, v84\n"
        "v_rcp_f32 v86, v86\n"
        "v_add_f32 v16, v16, v17\n"
        "v_add_f32 v20, v20, v21\n"
        "v_add_f32 v18, v18, v19\n"
        "v_mul_f32 v16, v16, v84\n"
        "v_mul_f32 v20, v20, v84\n"
        "v_add_f32 v22, v22, v23\n"
        "v_cvt_pk_bf16_f32 v17, v16, v20\n"
        "v_mul_f32 v18, v18, v86\n"
        "v_mul_f32 v22, v22, v86\n"
        "v_cvt_pk_bf16_f32 v19, v18, v22\n"
        "ds_write2_b32 v9, v17, v19 offset1:8\n"
        "v_mov_b32 v60, 0\n"
        "v_mov_b32 v62, 0\n"
        "v_mov_b32 v63, 0\n"
        "v_add_f32 v10, v24, v25\n"
        "v_add_f32 v11, v26, v27\n"
        "v_mfma_f32_16x16x32_bf16 v[32:35], v[60:63], v[60:63], 0\n"
        "v_add_f32 v16, v28, v29\n"
        "v_add_f32 v18, v30, v31\n"
        "v_mfma_f32_16x16x32_bf16 v[36:39], v[60:63], v[60:63], 0\n"
        "v_mfma_f32_16x16x32_bf16 v[40:43], v[60:63], v[60:63], 0\n"
        "s_waitcnt lgkmcnt(0)\n"
        "s_barrier\n"
        "ds_read_b128 v[24:27], v1 offset:28672\n"
        "v_mfma_f32_16x16x32_bf16 v[44:47], v[60:63], v[60:63], 0\n"
        "ds_read_b128 v[28:31], v1 offset:29696\n"
        "v_add_u32 v8, 0x10000, v6\n"
        "v_mov_b32 v12, v99\n"
        "v_mfma_f32_16x16x32_bf16 v[48:51], v[60:63], v[60:63], 0\n"
        "ds_read_b128 v[64:67], v8 offset:0\n"
        "v_mov_b32 v13, v103\n"
        "v_cvt_pk_bf16_f32 v14, v10, v11\n"
        "v_mfma_f32_16x16x32_bf16 v[52:55], v[60:63], v[60:63], 0\n"
        "ds_read_b128 v[68:71], v8 offset:1024\n"
        "v_cvt_pk_bf16_f32 v15, v16, v18\n"
        "s_waitcnt lgkmcnt(3)\n"
        "v_smfmac_f32_16x16x64_bf16 v[32:35], v[24:27], v[192:199], v0\n"
        "ds_read_b128 v[72:75], v8 offset:2048\n"
        "v_smfmac_f32_16x16x64_bf16 v[36:39], v[24:27], v[200:207], v0\n"
        "ds_read_b128 v[76:79], v8 offset:3072\n"
        "s_waitcnt lgkmcnt(4)\n"
        "v_smfmac_f32_16x16x64_bf16 v[32:35], v[28:31], v[208:215], v0\n"
        "ds_read_b128 v[80:83], v8 offset:16384\n"
        "v_mov_b32 v56, 0\n"
        "v_smfmac_f32_16x16x64_bf16 v[36:39], v[28:31], v[216:223], v0\n"
        "ds_read_b128 v[84:87], v8 offset:17408\n"
        "s_nop 0\n"
        "v_mov_b32 v57, 0\n"
        "v_smfmac_f32_16x16x64_bf16 v[40:43], v[24:27], v[224:231], v0\n"
        "ds_read_b128 v[88:91], v8 offset:18432\n"
        "s_nop 0\n"
        "v_add_f32 v32, v32, v33\n"
        "v_add_f32 v34, v34, v35\n"
        "v_add_f32 v36, v36, v37\n"
        "v_add_f32 v38, v38, v39\n"
        "v_cvt_pk_bf16_f32 v32, v32, v36\n"
        "v_cvt_pk_bf16_f32 v33, v34, v38\n"
        "v_smfmac_f32_16x16x64_bf16 v[44:47], v[24:27], v[232:239], v0\n"
        "global_store_dwordx2 v5, v[32:33], s[44:45] sc1\n"
        "v_smfmac_f32_16x16x64_bf16 v[40:43], v[28:31], v[240:247], v0\n"
        "ds_read_b128 v[92:95], v8 offset:19456\n"
        "ds_read_b128 v[96:99], v8 offset:32768\n"
        "v_smfmac_f32_16x16x64_bf16 v[44:47], v[28:31], v[248:255], v0\n"
        "ds_read_b128 v[100:103], v8 offset:33792\n"
        "s_waitcnt lgkmcnt(6)\n"
        "v_mov_b32 v58, 0\n"
        "v_smfmac_f32_16x16x64_bf16 v[48:51], v[24:27], v[64:71], v0\n"
        "ds_read_b128 v[104:107], v8 offset:34816\n"
        "s_nop 0\n"
        "v_add_f32 v40, v40, v41\n"
        "v_add_f32 v42, v42, v43\n"
        "v_add_f32 v44, v44, v45\n"
        "v_add_f32 v46, v46, v47\n"
        "v_cvt_pk_bf16_f32 v40, v40, v44\n"
        "v_cvt_pk_bf16_f32 v41, v42, v46\n"
        "v_smfmac_f32_16x16x64_bf16 v[52:55], v[24:27], v[72:79], v0\n"
        "global_store_dwordx2 v5, v[40:41], s[46:47] sc1\n"
        "s_waitcnt lgkmcnt(3)\n"
        "v_smfmac_f32_16x16x64_bf16 v[48:51], v[28:31], v[80:87], v0\n"
        "ds_read_b128 v[108:111], v8 offset:35840\n"
        "ds_read_b128 v[112:115], v8 offset:49152\n"
        "v_mov_b32 v59, 0\n"
        "v_smfmac_f32_16x16x64_bf16 v[52:55], v[28:31], v[88:95], v0\n"
        "ds_read_b128 v[116:119], v8 offset:50176\n"
        "s_waitcnt lgkmcnt(4)\n"
        "s_nop 0\n"
        "v_smfmac_f32_16x16x64_bf16 v[56:59], v[24:27], v[96:103], v0\n"
        "ds_read_b128 v[120:123], v8 offset:51200\n"
        "ds_read_b128 v[124:127], v8 offset:52224\n"
        "s_waitcnt lgkmcnt(4)\n"
        "v_add_f32 v48, v48, v49\n"
        "v_add_f32 v50, v50, v51\n"
        "v_add_f32 v52, v52, v53\n"
        "v_add_f32 v54, v54, v55\n"
        "v_cvt_pk_bf16_f32 v48, v48, v52\n"
        "v_cvt_pk_bf16_f32 v49, v50, v54\n"
        "v_smfmac_f32_16x16x64_bf16 v[60:63], v[24:27], v[104:111], v0\n"
        "global_store_dwordx2 v5, v[48:49], s[48:49] sc1\n"
        "s_waitcnt lgkmcnt(0)\n"
        "v_smfmac_f32_16x16x64_bf16 v[56:59], v[28:31], v[112:119], v0\n"
        "v_smfmac_f32_16x16x64_bf16 v[60:63], v[28:31], v[120:127], v0\n"
        "s_nop 6\n"
        "v_add_f32 v56, v56, v57\n"
        "v_add_f32 v58, v58, v59\n"
        "v_add_f32 v60, v60, v61\n"
        "v_add_f32 v62, v62, v63\n"
        "v_cvt_pk_bf16_f32 v56, v56, v60\n"
        "v_cvt_pk_bf16_f32 v57, v58, v62\n"
        "global_store_dwordx2 v5, v[56:57], s[50:51] sc1\n"
        "ds_read_b128 v[64:67], v6 offset:0\n"
        "ds_read_b128 v[68:71], v6 offset:1024\n"
        "ds_read_b128 v[72:75], v6 offset:2048\n"
        "ds_read_b128 v[76:79], v6 offset:3072\n"
        "ds_read_b128 v[80:83], v6 offset:16384\n"
        "ds_read_b128 v[84:87], v6 offset:17408\n"
        "ds_read_b128 v[88:91], v6 offset:18432\n"
        "ds_read_b128 v[92:95], v6 offset:19456\n"
        "ds_read_b128 v[96:99], v6 offset:32768\n"
        "ds_read_b128 v[100:103], v6 offset:33792\n"
        "ds_read_b128 v[104:107], v6 offset:34816\n"
        "ds_read_b128 v[108:111], v6 offset:35840\n"
        "ds_read_b128 v[112:115], v6 offset:49152\n"
        "ds_read_b128 v[116:119], v6 offset:50176\n"
        "ds_read_b128 v[120:123], v6 offset:51200\n"
        "ds_read_b128 v[124:127], v6 offset:52224\n"
        "v_mov_b32 v60, v12\n"
        "v_mov_b32 v61, v13\n"
        "v_mov_b32 v62, v14\n"
        "v_mov_b32 v63, v15\n"
        "v_mov_b32 v8, 0\n"
        "v_mov_b32 v9, 0\n"
        "v_mov_b32 v10, 0\n"
        "v_mov_b32 v11, 0\n"
        "v_mov_b32 v12, 0\n"
        "v_mov_b32 v13, 0\n"
        "v_mov_b32 v14, 0\n"
        "v_mov_b32 v15, 0\n"
        "v_mov_b32 v16, 0\n"
        "v_mov_b32 v17, 0\n"
        "v_mov_b32 v18, 0\n"
        "v_mov_b32 v19, 0\n"
        "v_mov_b32 v20, 0\n"
        "v_mov_b32 v21, 0\n"
        "v_mov_b32 v22, 0\n"
        "v_mov_b32 v23, 0\n"
        "v_mov_b32 v24, 0\n"
        "v_mov_b32 v25, 0\n"
        "v_mov_b32 v26, 0\n"
        "v_mov_b32 v27, 0\n"
        "v_mov_b32 v28, 0\n"
        "v_mov_b32 v29, 0\n"
        "v_mov_b32 v30, 0\n"
        "v_mov_b32 v31, 0\n"
        "s_mov_b32 m0, s37\n"
        "s_nop 0\n"
        "global_load_lds_dwordx4 v1, s[38:39] sc1\n"
        "global_load_lds_dwordx4 v1, s[38:39] offset:1024 sc1\n"
        "s_mov_b32 m0, s55\n"
        "s_nop 0\n"
        "global_load_lds_dwordx4 v1, s[40:41] sc1\n"
        "global_load_lds_dwordx4 v1, s[40:41] offset:1024 sc1\n"
        "s_xor_b32 s44, s44, 0x100000\n"
        "s_xor_b32 s46, s46, 0x100000\n"
        "s_xor_b32 s48, s48, 0x100000\n"
        "s_xor_b32 s50, s50, 0x100000\n"
        "s_add_u32 s38, s38, 0x40000\n"
        "s_addc_u32 s39, s39, 0\n"
        "s_add_u32 s40, s40, 0x40000\n"
        "s_addc_u32 s41, s41, 0\n"
        "s_mov_b64 s[96:97], s[52:53]\n"
        "s_add_u32 s54, s54, 1\n"
        "s_cmp_eq_u32 s54, 3\n"
        "s_cselect_b32 s54, 0, s54\n"
        "s_cselect_b32 s65, 0xfffe0000, s98\n"
        "s_add_u32 s52, s52, s65\n"
        "s_mov_b32 m0, 6144\n"
        "s_add_u32 s36, s36, 1\n"
        "s_waitcnt vmcnt(0) lgkmcnt(0)\n"
        "s_barrier\n"
        "s_nop 15\n"
        "s_nop 15\n"
        "s_nop 15\n"
        "s_nop 15\n"
        "s_nop 15\n"
        "s_nop 0\n"
        "global_load_lds_dwordx4 v1, s[96:97] sc1\n"
        "global_load_lds_dwordx4 v1, s[96:97] offset:1024 sc1\n"
        "s_mov_b64 s[82:83], s[42:43]\n"
        "s_add_u32 s42, s42, 0x20000\n"
        "s_addc_u32 s43, s43, 0\n"
        "s_branch Lw3step%=\n"
        "Lw0slow%=:\n"
        "v_max3_u32 v9, v32, v33, v34\n"
        "v_max3_u32 v9, v9, v35, v36\n"
        "v_max3_u32 v9, v9, v37, v38\n"
        "v_max3_u32 v9, v9, v39, v40\n"
        "v_max3_u32 v9, v9, v41, v42\n"
        "v_max3_u32 v9, v9, v43, v44\n"
        "v_max3_u32 v9, v9, v45, v46\n"
        "v_max3_u32 v9, v9, v47, v48\n"
        "v_max3_u32 v9, v9, v49, v50\n"
        "v_max3_u32 v9, v9, v51, v52\n"
        "v_max3_u32 v9, v9, v53, v54\n"
        "v_max3_u32 v9, v9, v55, v56\n"
        "v_max3_u32 v9, v9, v57, v58\n"
        "v_max3_u32 v9, v9, v59, v60\n"
        "v_max3_u32 v9, v9, v61, v62\n"
        "v_max_u32 v9, v9, v63\n"
        "v_cmp_eq_u32 vcc, -1, v9\n"
        "s_cbranch_vccz Lw0back%=\n"
        "Lw0again%=:\n"
        "s_add_u32 s67, s67, 1\n"
        "s_cmp_gt_u32 s67, 262144\n"
        "s_cbranch_scc1 Lfail%=\n"
        "s_waitcnt vmcnt(0) lgkmcnt(0)\n"
        "s_barrier\n"
        "global_load_lds_dwordx4 v1, s[96:97] sc1\n"
        "global_load_lds_dwordx4 v1, s[96:97] offset:1024 sc1\n"
        "s_memrealtime s[0:1]\n"
        "s_mov_b32 s63, 250\n"
        "s_add_u32 s99, s99, 1\n"
        "s_waitcnt lgkmcnt(0)\n"
        "s_lshl_b32 s0, s0, 3\n"
        "s_add_u32 s62, s0, s61\n"
        "s_add_u32 s65, s62, s63\n"
        "s_lshl_b32 s65, s65, 8\n"
        "s_add_u32 s65, s65, s36\n"
        "v_add_u32 v33, s65, 1\n"
        "s_mov_b64 exec, 1\n"
        "global_store_dword v1, v33, s[78:79] sc1\n"
        "s_mov_b64 exec, -1\n"
        "ds_read_b128 v[64:67], v6 offset:0\n"
        "ds_read_b128 v[68:71], v6 offset:1024\n"
        "ds_read_b128 v[72:75], v6 offset:2048\n"
        "ds_read_b128 v[76:79], v6 offset:3072\n"
        "ds_read_b128 v[80:83], v6 offset:16384\n"
        "ds_read_b128 v[84:87], v6 offset:17408\n"
        "ds_read_b128 v[88:91], v6 offset:18432\n"
        "ds_read_b128 v[92:95], v6 offset:19456\n"
        "ds_read_b128 v[96:99], v6 offset:32768\n"
        "ds_read_b128 v[100:103], v6 offset:33792\n"
        "ds_read_b128 v[104:107], v6 offset:34816\n"
        "ds_read_b128 v[108:111], v6 offset:35840\n"
        "ds_read_b128 v[112:115], v6 offset:49152\n"
        "ds_read_b128 v[116:119], v6 offset:50176\n"
        "ds_read_b128 v[120:123], v6 offset:51200\n"
        "ds_read_b128 v[124:127], v6 offset:52224\n"
        "v_mov_b32 v8, 0\n"
        "v_mov_b32 v9, 0\n"
        "v_mov_b32 v10, 0\n"
        "v_mov_b32 v11, 0\n"
        "v_mov_b32 v12, 0\n"
        "v_mov_b32 v13, 0\n"
        "v_mov_b32 v14, 0\n"
        "v_mov_b32 v15, 0\n"
        "v_mov_b32 v16, 0\n"
        "v_mov_b32 v17, 0\n"
        "v_mov_b32 v18, 0\n"
        "v_mov_b32 v19, 0\n"
        "v_mov_b32 v20, 0\n"
        "v_mov_b32 v21, 0\n"
        "v_mov_b32 v22, 0\n"
        "v_mov_b32 v23, 0\n"
        "v_mov_b32 v24, 0\n"
        "v_mov_b32 v25, 0\n"
        "v_mov_b32 v26, 0\n"
        "v_mov_b32 v27, 0\n"
        "v_mov_b32 v28, 0\n"
        "v_mov_b32 v29, 0\n"
        "v_mov_b32 v30, 0\n"
        "v_mov_b32 v31, 0\n"
        "s_waitcnt vmcnt(1) lgkmcnt(0)\n"
        "s_barrier\n"
        "ds_read_b128 v[32:35], v1 offset:0\n"
        "ds_read_b128 v[36:39], v1 offset:1024\n"
        "ds_read_b128 v[40:43], v1 offset:2048\n"
        "ds_read_b128 v[44:47], v1 offset:3072\n"
        "ds_read_b128 v[48:51], v1 offset:4096\n"
        "ds_read_b128 v[52:55], v1 offset:5120\n"
        "ds_read_b128 v[56:59], v1 offset:6144\n"
        "ds_read_b128 v[60:63], v1 offset:7168\n"
        "s_waitcnt lgkmcnt(0)\n"
        "v_max3_u32 v7, v32, v33, v34\n"
        "v_max3_u32 v7, v7, v35, v36\n"
        "v_max3_u32 v7, v7, v37, v38\n"
        "v_max3_u32 v7, v7, v39, v40\n"
        "v_max3_u32 v7, v7, v41, v42\n"
        "v_max3_u32 v7, v7, v43, v44\n"
        "v_max3_u32 v7, v7, v45, v46\n"
        "v_max3_u32 v7, v7, v47, v48\n"
        "v_max3_u32 v7, v7, v49, v50\n"
        "v_max3_u32 v7, v7, v51, v52\n"
        "v_max3_u32 v7, v7, v53, v54\n"
        "v_max3_u32 v7, v7, v55, v56\n"
        "v_max3_u32 v7, v7, v57, v58\n"
        "v_max3_u32 v7, v7, v59, v60\n"
        "v_max3_u32 v7, v7, v61, v62\n"
        "v_max_u32 v7, v7, v63\n"
        "v_cmp_eq_u32 vcc, -1, v7\n"
        "s_cbranch_vccnz Lw0again%=\n"
        "s_mov_b64 s[82:83], s[80:81]\n"
        "s_sub_u32 s65, s36, 1\n"
        "v_mov_b32 v59, s65\n"
        "s_branch Lw0in%=\n"
        "Llong%=:\n"
        "s_memrealtime s[0:1]\n"
        "s_waitcnt lgkmcnt(0)\n"
        "s_lshl_b32 s0, s0, 3\n"
        "s_sub_u32 s0, s64, s0\n"
        "s_cmp_gt_i32 s0, 130\n"
        "s_cbranch_scc1 Llong%=\n"
        "s_memrealtime s[0:1]\n"
        "s_nop 1\n"
        "s_memrealtime s[2:3]\n"
        "s_nop 1\n"
        "s_memrealtime s[4:5]\n"
        "s_nop 1\n"
        "s_memrealtime s[6:7]\n"
        "s_nop 1\n"
        "s_memrealtime s[8:9]\n"
        "s_nop 1\n"
        "s_memrealtime s[10:11]\n"
        "s_nop 1\n"
        "s_memrealtime s[12:13]\n"
        "s_nop 1\n"
        "s_memrealtime s[14:15]\n"
        "s_memtime s[68:69]\n"
        "s_waitcnt lgkmcnt(0)\n"
        "s_add_u32 s73, s0, s2\n"
        "s_add_u32 s73, s73, s4\n"
        "s_add_u32 s73, s73, s6\n"
        "s_add_u32 s73, s73, s8\n"
        "s_add_u32 s73, s73, s10\n"
        "s_add_u32 s73, s73, s12\n"
        "s_add_u32 s73, s73, s14\n"
        "s_branch Lshort%=\n"
        "Lrepoll%=:\n"
        "s_add_u32 s67, s67, 1\n"
        "s_cmp_gt_u32 s67, 262144\n"
        "s_cbranch_scc1 Lfail%=\n"
        "s_sleep 2\n"
        "v_lshl_add_u32 v7, v1, 3, s88\n"
        "v_writelane_b32 v7, s93, 32\n"
        "v_writelane_b32 v7, s94, 33\n"
        "s_mov_b64 exec, s[90:91]\n"
        "global_load_dword v7, v7, s[56:57] sc1\n"
        "s_mov_b64 exec, -1\n"
        "s_waitcnt vmcnt(0)\n"
        "s_branch Lchk%=\n"
        "Lw1slow%=:\n"
        "v_max3_u32 v9, v32, v33, v34\n"
        "v_max3_u32 v9, v9, v35, v36\n"
        "v_max3_u32 v9, v9, v37, v38\n"
        "v_max3_u32 v9, v9, v39, v40\n"
        "v_max3_u32 v9, v9, v41, v42\n"
        "v_max3_u32 v9, v9, v43, v44\n"
        "v_max3_u32 v9, v9, v45, v46\n"
        "v_max3_u32 v9, v9, v47, v48\n"
        "v_max3_u32 v9, v9, v49, v50\n"
        "v_max3_u32 v9, v9, v51, v52\n"
        "v_max3_u32 v9, v9, v53, v54\n"
        "v_max3_u32 v9, v9, v55, v56\n"
        "v_max3_u32 v9, v9, v57, v58\n"
        "v_max3_u32 v9, v9, v59, v60\n"
        "v_max3_u32 v9, v9, v61, v62\n"
        "v_max_u32 v9, v9, v63\n"
        "v_cmp_eq_u32 vcc, -1, v9\n"
        "s_cbranch_vccz Lw1back%=\n"
        "Lw1again%=:\n"
        "s_add_u32 s67, s67, 1\n"
        "s_cmp_gt_u32 s67, 262144\n"
        "s_cbranch_scc1 Lfail%=\n"
        "s_waitcnt vmcnt(0) lgkmcnt(0)\n"
        "s_barrier\n"
        "global_load_lds_dwordx4 v1, s[96:97] sc1\n"
        "global_load_lds_dwordx4 v1, s[96:97] offset:1024 sc1\n"
        "ds_read_b128 v[64:67], v6 offset:0\n"
        "ds_read_b128 v[68:71], v6 offset:1024\n"
        "ds_read_b128 v[72:75], v6 offset:2048\n"
        "ds_read_b128 v[76:79], v6 offset:3072\n"
        "ds_read_b128 v[80:83], v6 offset:16384\n"
        "ds_read_b128 v[84:87], v6 offset:17408\n"
        "ds_read_b128 v[88:91], v6 offset:18432\n"
        "ds_read_b128 v[92:95], v6 offset:19456\n"
        "ds_read_b128 v[96:99], v6 offset:32768\n"
        "ds_read_b128 v[100:103], v6 offset:33792\n"
        "ds_read_b128 v[104:107], v6 offset:34816\n"
        "ds_read_b128 v[108:111], v6 offset:35840\n"
        "ds_read_b128 v[112:115], v6 offset:49152\n"
        "ds_read_b128 v[116:119], v6 offset:50176\n"
        "ds_read_b128 v[120:123], v6 offset:51200\n"
        "ds_read_b128 v[124:127], v6 offset:52224\n"
        "v_mov_b32 v8, 0\n"
        "v_mov_b32 v9, 0\n"
        "v_mov_b32 v10, 0\n"
        "v_mov_b32 v11, 0\n"
        "v_mov_b32 v12, 0\n"
        "v_mov_b32 v13, 0\n"
        "v_mov_b32 v14, 0\n"
        "v_mov_b32 v15, 0\n"
        "v_mov_b32 v16, 0\n"
        "v_mov_b32 v17, 0\n"
        "v_mov_b32 v18, 0\n"
        "v_mov_b32 v19, 0\n"
        "v_mov_b32 v20, 0\n"
        "v_mov_b32 v21, 0\n"
        "v_mov_b32 v22, 0\n"
        "v_mov_b32 v23, 0\n"
        "v_mov_b32 v24, 0\n"
        "v_mov_b32 v25, 0\n"
        "v_mov_b32 v26, 0\n"
        "v_mov_b32 v27, 0\n"
        "v_mov_b32 v28, 0\n"
        "v_mov_b32 v29, 0\n"
        "v_mov_b32 v30, 0\n"
        "v_mov_b32 v31, 0\n"
        "s_waitcnt vmcnt(0) lgkmcnt(0)\n"
        "s_barrier\n"
        "ds_read_b128 v[32:35], v1 offset:0\n"
        "ds_read_b128 v[36:39], v1 offset:1024\n"
        "ds_read_b128 v[40:43], v1 offset:2048\n"
        "ds_read_b128 v[44:47], v1 offset:3072\n"
        "ds_read_b128 v[48:51], v1 offset:4096\n"
        "ds_read_b128 v[52:55], v1 offset:5120\n"
        "ds_read_b128 v[56:59], v1 offset:6144\n"
        "ds_read_b128 v[60:63], v1 offset:7168\n"
        "s_waitcnt lgkmcnt(0)\n"
        "v_max3_u32 v7, v32, v33, v34\n"
        "v_max3_u32 v7, v7, v35, v36\n"
        "v_max3_u32 v7, v7, v37, v38\n"
        "v_max3_u32 v7, v7, v39, v40\n"
        "v_max3_u32 v7, v7, v41, v42\n"
        "v_max3_u32 v7, v7, v43, v44\n"
        "v_max3_u32 v7, v7, v45, v46\n"
        "v_max3_u32 v7, v7, v47, v48\n"
        "v_max3_u32 v7, v7, v49, v50\n"
        "v_max3_u32 v7, v7, v51, v52\n"
        "v_max3_u32 v7, v7, v53, v54\n"
        "v_max3_u32 v7, v7, v55, v56\n"
        "v_max3_u32 v7, v7, v57, v58\n"
        "v_max3_u32 v7, v7, v59, v60\n"
        "v_max3_u32 v7, v7, v61, v62\n"
        "v_max_u32 v7, v7, v63\n"
        "v_cmp_eq_u32 vcc, -1, v7\n"
        "s_cbranch_vccnz Lw1again%=\n"
        "s_mov_b64 s[82:83], s[80:81]\n"
        "s_sub_u32 s65, s36, 1\n"
        "v_mov_b32 v59, s65\n"
        "s_branch Lw1step%=\n"
        "Lw2slow%=:\n"
        "v_max3_u32 v9, v32, v33, v34\n"
        "v_max3_u32 v9, v9, v35, v36\n"
        "v_max3_u32 v9, v9, v37, v38\n"
        "v_max3_u32 v9, v9, v39, v40\n"
        "v_max3_u32 v9, v9, v41, v42\n"
        "v_max3_u32 v9, v9, v43, v44\n"
        "v_max3_u32 v9, v9, v45, v46\n"
        "v_max3_u32 v9, v9, v47, v48\n"
        "v_max3_u32 v9, v9, v49, v50\n"
        "v_max3_u32 v9, v9, v51, v52\n"
        "v_max3_u32 v9, v9, v53, v54\n"
        "v_max3_u32 v9, v9, v55, v56\n"
        "v_max3_u32 v9, v9, v57, v58\n"
        "v_max3_u32 v9, v9, v59, v60\n"
        "v_max3_u32 v9, v9, v61, v62\n"
        "v_max_u32 v9, v9, v63\n"
        "v_cmp_eq_u32 vcc, -1, v9\n"
        "s_cbranch_vccz Lw2back%=\n"
        "Lw2again%=:\n"
        "s_add_u32 s67, s67, 1\n"
        "s_cmp_gt_u32 s67, 262144\n"
        "s_cbranch_scc1 Lfail%=\n"
        "s_waitcnt vmcnt(0) lgkmcnt(0)\n"
        "s_barrier\n"
        "global_load_lds_dwordx4 v1, s[96:97] sc1\n"
        "global_load_lds_dwordx4 v1, s[96:97] offset:1024 sc1\n"
        "ds_read_b128 v[64:67], v6 offset:0\n"
        "ds_read_b128 v[68:71], v6 offset:1024\n"
        "ds_read_b128 v[72:75], v6 offset:2048\n"
        "ds_read_b128 v[76:79], v6 offset:3072\n"
        "ds_read_b128 v[80:83], v6 offset:16384\n"
        "ds_read_b128 v[84:87], v6 offset:17408\n"
        "ds_read_b128 v[88:91], v6 offset:18432\n"
        "ds_read_b128 v[92:95], v6 offset:19456\n"
        "ds_read_b128 v[96:99], v6 offset:32768\n"
        "ds_read_b128 v[100:103], v6 offset:33792\n"
        "ds_read_b128 v[104:107], v6 offset:34816\n"
        "ds_read_b128 v[108:111], v6 offset:35840\n"
        "ds_read_b128 v[112:115], v6 offset:49152\n"
        "ds_read_b128 v[116:119], v6 offset:50176\n"
        "ds_read_b128 v[120:123], v6 offset:51200\n"
        "ds_read_b128 v[124:127], v6 offset:52224\n"
        "v_mov_b32 v8, 0\n"
        "v_mov_b32 v9, 0\n"
        "v_mov_b32 v10, 0\n"
        "v_mov_b32 v11, 0\n"
        "v_mov_b32 v12, 0\n"
        "v_mov_b32 v13, 0\n"
        "v_mov_b32 v14, 0\n"
        "v_mov_b32 v15, 0\n"
        "v_mov_b32 v16, 0\n"
        "v_mov_b32 v17, 0\n"
        "v_mov_b32 v18, 0\n"
        "v_mov_b32 v19, 0\n"
        "v_mov_b32 v20, 0\n"
        "v_mov_b32 v21, 0\n"
        "v_mov_b32 v22, 0\n"
        "v_mov_b32 v23, 0\n"
        "v_mov_b32 v24, 0\n"
        "v_mov_b32 v25, 0\n"
        "v_mov_b32 v26, 0\n"
        "v_mov_b32 v27, 0\n"
        "v_mov_b32 v28, 0\n"
        "v_mov_b32 v29, 0\n"
        "v_mov_b32 v30, 0\n"
        "v_mov_b32 v31, 0\n"
        "s_waitcnt vmcnt(0) lgkmcnt(0)\n"
        "s_barrier\n"
        "ds_read_b128 v[32:35], v1 offset:0\n"
        "ds_read_b128 v[36:39], v1 offset:1024\n"
        "ds_read_b128 v[40:43], v1 offset:2048\n"
        "ds_read_b128 v[44:47], v1 offset:3072\n"
        "ds_read_b128 v[48:51], v1 offset:4096\n"
        "ds_read_b128 v[52:55], v1 offset:5120\n"
        "ds_read_b128 v[56:59], v1 offset:6144\n"
        "ds_read_b128 v[60:63], v1 offset:7168\n"
        "s_waitcnt lgkmcnt(0)\n"
        "v_max3_u32 v7, v32, v33, v34\n"
        "v_max3_u32 v7, v7, v35, v36\n"
        "v_max3_u32 v7, v7, v37, v38\n"
        "v_max3_u32 v7, v7, v39, v40\n"
        "v_max3_u32 v7, v7, v41, v42\n"
        "v_max3_u32 v7, v7, v43, v44\n"
        "v_max3_u32 v7, v7, v45, v46\n"
        "v_max3_u32 v7, v7, v47, v48\n"
        "v_max3_u32 v7, v7, v49, v50\n"
        "v_max3_u32 v7, v7, v51, v52\n"
        "v_max3_u32 v7, v7, v53, v54\n"
        "v_max3_u32 v7, v7, v55, v56\n"
        "v_max3_u32 v7, v7, v57, v58\n"
        "v_max3_u32 v7, v7, v59, v60\n"
        "v_max3_u32 v7, v7, v61, v62\n"
        "v_max_u32 v7, v7, v63\n"
        "v_cmp_eq_u32 vcc, -1, v7\n"
        "s_cbranch_vccnz Lw2again%=\n"
        "s_mov_b64 s[82:83], s[80:81]\n"
        "s_sub_u32 s65, s36, 1\n"
        "v_mov_b32 v59, s65\n"
        "s_branch Lw2step%=\n"
        "Lw3slow%=:\n"
        "v_max3_u32 v9, v32, v33, v34\n"
        "v_max3_u32 v9, v9, v35, v36\n"
        "v_max3_u32 v9, v9, v37, v38\n"
        "v_max3_u32 v9, v9, v39, v40\n"
        "v_max3_u32 v9, v9, v41, v42\n"
        "v_max3_u32 v9, v9, v43, v44\n"
        "v_max3_u32 v9, v9, v45, v46\n"
        "v_max3_u32 v9, v9, v47, v48\n"
        "v_max3_u32 v9, v9, v49, v50\n"
        "v_max3_u32 v9, v9, v51, v52\n"
        "v_max3_u32 v9, v9, v53, v54\n"
        "v_max3_u32 v9, v9, v55, v56\n"
        "v_max3_u32 v9, v9, v57, v58\n"
        "v_max3_u32 v9, v9, v59, v60\n"
        "v_max3_u32 v9, v9, v61, v62\n"
        "v_max_u32 v9, v9, v63\n"
        "v_cmp_eq_u32 vcc, -1, v9\n"
        "s_cbranch_vccz Lw3back%=\n"
        "Lw3again%=:\n"
        "s_add_u32 s67, s67, 1\n"
        "s_cmp_gt_u32 s67, 262144\n"
        "s_cbranch_scc1 Lfail%=\n"
        "s_waitcnt vmcnt(0) lgkmcnt(0)\n"
        "s_barrier\n"
        "global_load_lds_dwordx4 v1, s[96:97] sc1\n"
        "global_load_lds_dwordx4 v1, s[96:97] offset:1024 sc1\n"
        "ds_read_b128 v[64:67], v6 offset:0\n"
        "ds_read_b128 v[68:71], v6 offset:1024\n"
        "ds_read_b128 v[72:75], v6 offset:2048\n"
        "ds_read_b128 v[76:79], v6 offset:3072\n"
        "ds_read_b128 v[80:83], v6 offset:16384\n"
        "ds_read_b128 v[84:87], v6 offset:17408\n"
        "ds_read_b128 v[88:91], v6 offset:18432\n"
        "ds_read_b128 v[92:95], v6 offset:19456\n"
        "ds_read_b128 v[96:99], v6 offset:32768\n"
        "ds_read_b128 v[100:103], v6 offset:33792\n"
        "ds_read_b128 v[104:107], v6 offset:34816\n"
        "ds_read_b128 v[108:111], v6 offset:35840\n"
        "ds_read_b128 v[112:115], v6 offset:49152\n"
        "ds_read_b128 v[116:119], v6 offset:50176\n"
        "ds_read_b128 v[120:123], v6 offset:51200\n"
        "ds_read_b128 v[124:127], v6 offset:52224\n"
        "v_mov_b32 v8, 0\n"
        "v_mov_b32 v9, 0\n"
        "v_mov_b32 v10, 0\n"
        "v_mov_b32 v11, 0\n"
        "v_mov_b32 v12, 0\n"
        "v_mov_b32 v13, 0\n"
        "v_mov_b32 v14, 0\n"
        "v_mov_b32 v15, 0\n"
        "v_mov_b32 v16, 0\n"
        "v_mov_b32 v17, 0\n"
        "v_mov_b32 v18, 0\n"
        "v_mov_b32 v19, 0\n"
        "v_mov_b32 v20, 0\n"
        "v_mov_b32 v21, 0\n"
        "v_mov_b32 v22, 0\n"
        "v_mov_b32 v23, 0\n"
        "v_mov_b32 v24, 0\n"
        "v_mov_b32 v25, 0\n"
        "v_mov_b32 v26, 0\n"
        "v_mov_b32 v27, 0\n"
        "v_mov_b32 v28, 0\n"
        "v_mov_b32 v29, 0\n"
        "v_mov_b32 v30, 0\n"
        "v_mov_b32 v31, 0\n"
        "s_waitcnt vmcnt(0) lgkmcnt(0)\n"
        "s_barrier\n"
        "ds_read_b128 v[32:35], v1 offset:0\n"
        "ds_read_b128 v[36:39], v1 offset:1024\n"
        "ds_read_b128 v[40:43], v1 offset:2048\n"
        "ds_read_b128 v[44:47], v1 offset:3072\n"
        "ds_read_b128 v[48:51], v1 offset:4096\n"
        "ds_read_b128 v[52:55], v1 offset:5120\n"
        "ds_read_b128 v[56:59], v1 offset:6144\n"
        "ds_read_b128 v[60:63], v1 offset:7168\n"
        "s_waitcnt lgkmcnt(0)\n"
        "v_max3_u32 v7, v32, v33, v34\n"
        "v_max3_u32 v7, v7, v35, v36\n"
        "v_max3_u32 v7, v7, v37, v38\n"
        "v_max3_u32 v7, v7, v39, v40\n"
        "v_max3_u32 v7, v7, v41, v42\n"
        "v_max3_u32 v7, v7, v43, v44\n"
        "v_max3_u32 v7, v7, v45, v46\n"
        "v_max3_u32 v7, v7, v47, v48\n"
        "v_max3_u32 v7, v7, v49, v50\n"
        "v_max3_u32 v7, v7, v51, v52\n"
        "v_max3_u32 v7, v7, v53, v54\n"
        "v_max3_u32 v7, v7, v55, v56\n"
        "v_max3_u32 v7, v7, v57, v58\n"
        "v_max3_u32 v7, v7, v59, v60\n"
        "v_max3_u32 v7, v7, v61, v62\n"
        "v_max_u32 v7, v7, v63\n"
        "v_cmp_eq_u32 vcc, -1, v7\n"
        "s_cbranch_vccnz Lw3again%=\n"
        "s_mov_b64 s[82:83], s[80:81]\n"
        "s_sub_u32 s65, s36, 1\n"
        "v_mov_b32 v59, s65\n"
        "s_branch Lw3step%=\n"
        "Lfail%=:\n"
        "s_mov_b32 s99, -1\n"
        "Llast%=:\n"
        "v_smfmac_f32_16x16x64_bf16 v[24:27], v[36:39], a[144:151], v0\n"
        "v_smfmac_f32_16x16x64_bf16 v[28:31], v[36:39], a[152:159], v0\n"
        "v_smfmac_f32_16x16x64_bf16 v[24:27], v[40:43], a[160:167], v0\n"
        "v_smfmac_f32_16x16x64_bf16 v[28:31], v[40:43], a[168:175], v0\n"
        "v_smfmac_f32_16x16x64_bf16 v[24:27], v[44:47], a[176:183], v0\n"
        "v_smfmac_f32_16x16x64_bf16 v[28:31], v[44:47], a[184:191], v0\n"
        "v_smfmac_f32_16x16x64_bf16 v[24:27], v[48:51], a[192:199], v0\n"
        "v_smfmac_f32_16x16x64_bf16 v[28:31], v[48:51], a[200:207], v0\n"
        "v_smfmac_f32_16x16x64_bf16 v[24:27], v[52:55], a[208:215], v0\n"
        "v_smfmac_f32_16x16x64_bf16 v[28:31], v[52:55], a[216:223], v0\n"
        "v_smfmac_f32_16x16x64_bf16 v[24:27], v[56:59], a[224:231], v0\n"
        "v_smfmac_f32_16x16x64_bf16 v[28:31], v[56:59], a[232:239], v0\n"
        "v_smfmac_f32_16x16x64_bf16 v[24:27], v[60:63], a[240:247], v0\n"
        "v_smfmac_f32_16x16x64_bf16 v[28:31], v[60:63], a[248:255], v0\n"
        "v_mov_b32 v12, v99\n"
        "v_mov_b32 v13, v103\n"
        "s_nop 4\n"
        "v_add_f32 v10, v24, v25\n"
        "v_add_f32 v11, v26, v27\n"
        "v_add_f32 v16, v28, v29\n"
        "v_add_f32 v18, v30, v31\n"
        "v_cvt_pk_bf16_f32 v14, v10, v11\n"
        "v_cvt_pk_bf16_f32 v15, v16, v18\n"
        "global_store_dwordx4 v1, v[12:15], s[42:43] sc1\n"
        "v_mov_b32 v8, 64\n"
        "s_waitcnt vmcnt(0) lgkmcnt(0)\n"
        "s_barrier\n"
        "s_mov_b64 exec, 1\n"
        "s_cmp_eq_u32 s35, 0\n"
        "s_cbranch_scc0 Lnf%=\n"
        "global_store_dword v1, v8, s[76:77] sc1\n"
        "Lnf%=:\n"
        "s_endpgm\n"
        ".Lrmt_end:\n"
        :: [h] "s"(head_of(id)), [id] "s"(id), [w] "s"(w), [ka] "s"(__builtin_amdgcn_kernarg_segment_ptr()) : "s34", "s35", "s97", "s98", "s99", "v255", "a255", "memory");
    __builtin_unreachable();
}

__device__ __forceinline__ void red_role(const Params& p, unsigned seen, int id, int w, int l) {
    GAS unsigned* rdy = (GAS unsigned*)(p.ws + F_RDY);
    unsigned fx = 0;
    if ((id & 3) == 0) {
        unsigned k = 0;
        if (w == 0 && l == 0) {
            u8* a = p.win + (7 << 15) + (3 << 18) + (1 << 20) + id * 64;
            auto rt = [&](unsigned off) {
                unsigned long long c0, c1;
                asm volatile("s_memtime %0\n\tglobal_store_dword %2, %3, %4 sc1\n\ts_waitcnt vmcnt(0)\n\ts_memtime %1\n\ts_waitcnt lgkmcnt(0)" : "=&s"(c0), "=&s"(c1) : "v"(off), "v"(-1), "s"(a) : "memory");
                return (unsigned)(c1 - c0);
            };
            rt(0);
            unsigned m0 = ~0u, m1 = ~0u;
            for (int i = 0; i < 2; ++i) { m0 = min(m0, rt(0)); m1 = min(m1, rt(0x2000)); }
            k = m1 < m0;
            unsigned n0 = min(m0, m1), n1 = ~0u;
            for (int i = 0; i < 3; ++i) { n1 = min(n1, rt((k << 13) ^ 0x4000)); if (i < 1) n0 = min(n0, rt(k << 13)); }
            k += n1 < n0 ? 2 : 0;
        }
        if (w == 0) tick_wait(p, seen);
        __syncthreads();
        if (w == 0 && l == 0) __hip_atomic_fetch_add(rdy, 1u << (8 * k), RLX_AGENT);
    }
    if (1) {
        const bool v = (id & 3) == 0;
        const int h = v ? id | 15 : (id & 28) + 1;
        for (int c = v ? 10 + ((id >> 2) & 3) : 2 + (id & 3); c < 24 && (!v || id >= 16); c += v ? 4 : 3) {
            const u8* a = (const u8*)((c < 16 ? (c & 1) ? p.Wv : p.Wk : p.Wq) + (size_t)((c < 16 ? c >> 1 : (c + 4) & 7) * 64 + w * 16 + (l >> 4)) * 4096 + h * 128) + (l & 15) * 16;
#pragma unroll
            for (int i = 0; i < 4; ++i) glds<0>(a + i * 32768, 0x24000);
        }
    }
    if (1 > 1) {
        for (int u = (id & 15) * 4 + w; u < (1 > 2 ? 768 : 512); u += 64) {
            const u8* a = (u < 512 ? (const u8*)(p.Wo + (size_t)((id & 16) * 128 + u * 4) * 512) : (const u8*)(p.Wq + (size_t)(u - 512) * 4096 + (id & 16) * 128)) + l * 16;
#pragma unroll
            for (int i = 0; i < 4; ++i) glds<0>(a + i * 1024, 0x24000);
        }
    }
    if (id & 3) {
        if (w == 0) tick_wait(p, seen);
        __syncthreads();
    }
    tick_ack(p);
#pragma nounroll
    for (int sp = 0; sp < SPIN_CAP; ++sp) { if (__hip_atomic_load(rdy + 1, RLX_AGENT) == 32) break; __builtin_amdgcn_s_sleep(24); }
    fx = pick(__builtin_amdgcn_readfirstlane(__hip_atomic_load(rdy, RLX_AGENT)));
    const int pp = l & 15;
    const unsigned reg = ((id >> 2) << 15) + (id & 3) * 2048, rl = reg + l * 4, pz = reg + l * 16;
    const unsigned mst = ((l >> 4) & 1) * 32768 + w * 2048 + id * 64 + (2 * (pp & 1) + (pp >> 3)) * 16 + ((pp >> 1) & 3) * 4;
    const unsigned stl = (l < 32 ? l : 34) * 128, idx = (l & 1) ? 0xDDDDu : 0x8888u;
    u8* pb = p.win + (w << 18) + (fx ^ (w == 2 ? 0x4000 : 0));
    u8* mb = p.win + (3 << 18) + (fx ^ 0x4000);
    u8* sb = mb + 6 * 32768;
    u8* kvp = p.win + (w < 2 ? O_KX : O_VX) + 262144 + head_of(id) * 8192 + (w & 1) * 4096;
    const int DA = w == 0 ? p.DA0 : w == 1 ? p.DA1 : w == 2 ? p.DA2 : p.DA3;
    const int agg = __builtin_amdgcn_readfirstlane(1 && id == 1 && w == 3);
    unsigned o0, o2;
    asm volatile(
        "s_mov_b32 s36, 1\n"
        "s_mov_b32 s67, 0\n"
        "s_mov_b32 s99, 0\n"
        "s_mov_b64 s[44:45], %[pb]\n"
        "s_mov_b64 s[48:49], %[kv]\n"
        "s_mov_b64 s[52:53], %[mb]\n"
        "s_add_u32 s96, s52, 0x20000\n"
        "s_mov_b32 s97, s53\n"
        "s_mov_b32 s54, 0\n"
        "s_mov_b32 s95, %[DB]\n"
        "s_mov_b32 s92, s95\n"
        "s_mov_b32 s93, 1\n"
        "s_mov_b32 s43, 250\n"
        "s_mov_b32 s98, 0\n"
        "s_add_u32 s50, s52, 0x38000\n"
        "s_mov_b32 s51, s53\n"
        "v_mov_b32 v56, 0x3f803f80\n"
        "v_mov_b32 v57, 0x3f803f80\n"
        "v_mov_b32 v58, 0x3f803f80\n"
        "v_mov_b32 v59, 0x3f803f80\n"
        "v_mov_b32 v60, -1\n"
        "v_mov_b32 v61, -1\n"
        "v_mov_b32 v62, -1\n"
        "v_mov_b32 v63, -1\n"
        "Lstep%=:\n"
        "global_load_dword v40, %[stl], %[sb] sc1\n"
        "s_waitcnt vmcnt(0)\n"
        "Lhave%=:\n"
        "s_memrealtime s[62:63]\n"
        "s_memtime s[68:69]\n"
        "v_mov_b32 v52, 0\n"
        "v_mov_b32 v53, 0\n"
        "v_mov_b32 v54, 0\n"
        "v_mov_b32 v55, 0\n"
        "s_waitcnt lgkmcnt(0)\n"
        "s_lshl_b32 s62, s62, 3\n"
        "v_and_b32 v42, 0xff, v40\n"
        "v_cmp_gt_u32 vcc, s36, v42\n"
        "v_readlane_b32 s65, v40, 34\n"
        "s_cmp_lg_u32 vcc_lo, 0\n"
        "s_cbranch_scc1 Lagain%=\n"
        "s_cmp_eq_u32 s65, 0\n"
        "s_cselect_b32 s95, s95, s65\n"
        "s_cmp_eq_u32 s98, 0\n"
        "s_cbranch_scc0 Lnp%=\n"
        "s_mov_b32 s98, 1\n"
        "global_load_dword v39, %[l64], s[48:49] sc1\n"
        "s_add_u32 s48, s48, 0x40000\n"
        "s_addc_u32 s49, s49, 0\n"
        "Lnp%=:\n"
        "s_lshl_b32 s65, s62, 8\n"
        "v_cmp_eq_u32 vcc, s36, v42\n"
        "v_subrev_u32 v40, s65, v40\n"
        "v_bfrev_b32 v42, 1\n"
        "v_cndmask_b32 v40, v42, v40, vcc\n"
        "s_nop 1\n"
        "v_max_i32_dpp v40, v40, v40 row_ror:8 row_mask:0xf bank_mask:0xf\n"
        "s_nop 1\n"
        "v_max_i32_dpp v40, v40, v40 row_ror:4 row_mask:0xf bank_mask:0xf\n"
        "s_nop 1\n"
        "v_max_i32_dpp v40, v40, v40 row_ror:2 row_mask:0xf bank_mask:0xf\n"
        "s_nop 1\n"
        "v_max_i32_dpp v40, v40, v40 row_ror:1 row_mask:0xf bank_mask:0xf\n"
        "s_nop 0\n"
        "v_readlane_b32 s64, v40, 0\n"
        "v_readlane_b32 s65, v40, 16\n"
        "s_max_i32 s64, s64, s65\n"
        "s_ashr_i32 s64, s64, 8\n"
        "s_add_u32 s64, s64, s62\n"
        "s_add_u32 s63, s64, %[DA]\n"
        "Lc%=:\n"
        "s_memrealtime s[76:77]\n"
        "s_waitcnt lgkmcnt(0)\n"
        "s_lshl_b32 s76, s76, 3\n"
        "s_sub_u32 s76, s63, s76\n"
        "s_cmp_gt_i32 s76, 130\n"
        "s_cbranch_scc1 Lc%=\n"
        "s_cmp_lt_i32 s76, 0\n"
        "s_cselect_b32 s93, 1, s93\n"
        "s_cbranch_scc1 Lpac%=\n"
        "s_memrealtime s[76:77]\n"
        "s_nop 1\n"
        "s_memrealtime s[78:79]\n"
        "s_nop 1\n"
        "s_memrealtime s[80:81]\n"
        "s_nop 1\n"
        "s_memrealtime s[82:83]\n"
        "s_nop 1\n"
        "s_memrealtime s[84:85]\n"
        "s_nop 1\n"
        "s_memrealtime s[86:87]\n"
        "s_nop 1\n"
        "s_memrealtime s[88:89]\n"
        "s_nop 1\n"
        "s_memrealtime s[90:91]\n"
        "s_memtime s[68:69]\n"
        "s_waitcnt lgkmcnt(0)\n"
        "s_add_u32 s62, s76, s78\n"
        "s_add_u32 s62, s62, s80\n"
        "s_add_u32 s62, s62, s82\n"
        "s_add_u32 s62, s62, s84\n"
        "s_add_u32 s62, s62, s86\n"
        "s_add_u32 s62, s62, s88\n"
        "s_add_u32 s62, s62, s90\n"
        "s_sub_u32 s65, s63, s62\n"
        "s_mul_i32 s65, s65, 3\n"
        "s_add_u32 s65, s65, s68\n"
        "Lpaa%=:\n"
        "s_memtime s[70:71]\n"
        "s_getpc_b64 s[74:75]\n"
        "Lpab%=:\n"
        "s_waitcnt lgkmcnt(0)\n"
        "s_sub_u32 s66, s65, s70\n"
        "s_cmp_gt_i32 s66, 127\n"
        "s_cbranch_scc1 Lpaa%=\n"
        "s_max_i32 s66, s66, 0\n"
        "s_andn2_b32 s66, s66, 3\n"
        "s_sub_u32 s66, Lpac%=-Lpab%=, s66\n"
        "s_add_u32 s74, s74, s66\n"
        "s_addc_u32 s75, s75, 0\n"
        "s_setpc_b64 s[74:75]\n"
        "s_nop 0\n"
        "s_nop 0\n"
        "s_nop 0\n"
        "s_nop 0\n"
        "s_nop 0\n"
        "s_nop 0\n"
        "s_nop 0\n"
        "s_nop 0\n"
        "s_nop 0\n"
        "s_nop 0\n"
        "s_nop 0\n"
        "s_nop 0\n"
        "s_nop 0\n"
        "s_nop 0\n"
        "s_nop 0\n"
        "s_nop 0\n"
        "s_nop 0\n"
        "s_nop 0\n"
        "s_nop 0\n"
        "s_nop 0\n"
        "s_nop 0\n"
        "s_nop 0\n"
        "s_nop 0\n"
        "s_nop 0\n"
        "s_nop 0\n"
        "s_nop 0\n"
        "s_nop 0\n"
        "s_nop 0\n"
        "s_nop 0\n"
        "s_nop 0\n"
        "s_nop 0\n"
        "Lpac%=:\n"
        "global_load_dword v44, %[rl], s[44:45] offset:0 sc1\n"
        "global_load_dword v45, %[rl], s[44:45] offset:256 sc1\n"
        "global_load_dword v46, %[rl], s[44:45] offset:512 sc1\n"
        "global_load_dword v47, %[rl], s[44:45] offset:768 sc1\n"
        "global_load_dword v48, %[rl], s[44:45] offset:1024 sc1\n"
        "global_load_dword v49, %[rl], s[44:45] offset:1280 sc1\n"
        "global_load_dword v50, %[rl], s[44:45] offset:1536 sc1\n"
        "global_load_dword v51, %[rl], s[44:45] offset:1792 sc1\n"
        "s_waitcnt vmcnt(0)\n"
        "v_smfmac_f32_16x16x64_bf16 v[52:55], v[56:59], v[44:51], %[idx]\n"
        "v_max3_u32 v42, v44, v45, v46\n"
        "v_max3_u32 v42, v42, v47, v48\n"
        "v_max3_u32 v42, v42, v49, v50\n"
        "v_max_u32 v42, v42, v51\n"
        "v_cmp_eq_u32 vcc, -1, v42\n"
        "s_cbranch_vccnz Lmiss%=\n"
        "s_mov_b64 exec, 0xffffffff\n"
        "s_nop 0\n"
        "v_cvt_pk_bf16_f32 v52, v52, v53\n"
        "global_store_dword %[mst], v52, s[52:53] sc1\n"
        "s_memrealtime s[76:77]\n"
        "s_nop 1\n"
        "s_memrealtime s[78:79]\n"
        "s_nop 1\n"
        "s_memrealtime s[80:81]\n"
        "s_nop 1\n"
        "s_memrealtime s[82:83]\n"
        "s_nop 1\n"
        "s_memrealtime s[84:85]\n"
        "s_nop 1\n"
        "s_memrealtime s[86:87]\n"
        "s_nop 1\n"
        "s_memrealtime s[88:89]\n"
        "s_nop 1\n"
        "s_memrealtime s[90:91]\n"
        "s_memtime s[68:69]\n"
        "s_mov_b64 exec, -1\n"
        "s_cmp_eq_u32 s36, 63\n"
        "s_cbranch_scc1 Ldone%=\n"
        "s_waitcnt lgkmcnt(0)\n"
        "s_add_u32 s62, s76, s78\n"
        "s_add_u32 s62, s62, s80\n"
        "s_add_u32 s62, s62, s82\n"
        "s_add_u32 s62, s62, s84\n"
        "s_add_u32 s62, s62, s86\n"
        "s_add_u32 s62, s62, s88\n"
        "s_add_u32 s62, s62, s90\n"
        "s_sub_u32 s65, s62, s64\n"
        "s_add_u32 s65, s65, %[CJ]\n"
        "s_ashr_i32 s65, s65, 1\n"
        "s_max_i32 s65, s65, 1\n"
        "s_min_i32 s65, s65, 255\n"
        "s_cmp_eq_u32 s93, 0\n"
        "s_cselect_b32 s65, s65, 0\n"
        "v_mov_b32 v43, s65\n"
        "s_mov_b32 s93, 0\n"
        "s_add_u32 s64, s64, s95\n"
        "s_add_u32 s64, s64, s43\n"
        "s_mov_b32 s43, 0\n"
        "s_add_u32 s63, s64, %[DP]\n"
        "s_sub_u32 s65, s63, s62\n"
        "s_mul_i32 s65, s65, 3\n"
        "s_add_u32 s65, s65, s68\n"
        "Lppa%=:\n"
        "s_memtime s[70:71]\n"
        "s_getpc_b64 s[74:75]\n"
        "Lppb%=:\n"
        "s_waitcnt lgkmcnt(0)\n"
        "s_sub_u32 s66, s65, s70\n"
        "s_cmp_gt_i32 s66, 127\n"
        "s_cbranch_scc1 Lppa%=\n"
        "s_max_i32 s66, s66, 0\n"
        "s_andn2_b32 s66, s66, 3\n"
        "s_sub_u32 s66, Lppc%=-Lppb%=, s66\n"
        "s_add_u32 s74, s74, s66\n"
        "s_addc_u32 s75, s75, 0\n"
        "s_setpc_b64 s[74:75]\n"
        "s_nop 0\n"
        "s_nop 0\n"
        "s_nop 0\n"
        "s_nop 0\n"
        "s_nop 0\n"
        "s_nop 0\n"
        "s_nop 0\n"
        "s_nop 0\n"
        "s_nop 0\n"
        "s_nop 0\n"
        "s_nop 0\n"
        "s_nop 0\n"
        "s_nop 0\n"
        "s_nop 0\n"
        "s_nop 0\n"
        "s_nop 0\n"
        "s_nop 0\n"
        "s_nop 0\n"
        "s_nop 0\n"
        "s_nop 0\n"
        "s_nop 0\n"
        "s_nop 0\n"
        "s_nop 0\n"
        "s_nop 0\n"
        "s_nop 0\n"
        "s_nop 0\n"
        "s_nop 0\n"
        "s_nop 0\n"
        "s_nop 0\n"
        "s_nop 0\n"
        "s_nop 0\n"
        "Lppc%=:\n"
        "global_load_dword v40, %[stl], %[sb] sc1\n"
        "s_cmp_eq_u32 %[agg], 0\n"
        "s_cbranch_scc1 Lna%=\n"
        "global_load_dword v41, %[stl], s[50:51] sc1\n"
        "Lna%=:\n"
        "s_add_u32 s63, s64, %[DZ]\n"
        "s_sub_u32 s65, s63, s62\n"
        "s_mul_i32 s65, s65, 3\n"
        "s_add_u32 s65, s65, s68\n"
        "Lpza%=:\n"
        "s_memtime s[70:71]\n"
        "s_getpc_b64 s[74:75]\n"
        "Lpzb%=:\n"
        "s_waitcnt lgkmcnt(0)\n"
        "s_sub_u32 s66, s65, s70\n"
        "s_cmp_gt_i32 s66, 127\n"
        "s_cbranch_scc1 Lpza%=\n"
        "s_max_i32 s66, s66, 0\n"
        "s_andn2_b32 s66, s66, 3\n"
        "s_sub_u32 s66, Lpzc%=-Lpzb%=, s66\n"
        "s_add_u32 s74, s74, s66\n"
        "s_addc_u32 s75, s75, 0\n"
        "s_setpc_b64 s[74:75]\n"
        "s_nop 0\n"
        "s_nop 0\n"
        "s_nop 0\n"
        "s_nop 0\n"
        "s_nop 0\n"
        "s_nop 0\n"
        "s_nop 0\n"
        "s_nop 0\n"
        "s_nop 0\n"
        "s_nop 0\n"
        "s_nop 0\n"
        "s_nop 0\n"
        "s_nop 0\n"
        "s_nop 0\n"
        "s_nop 0\n"
        "s_nop 0\n"
        "s_nop 0\n"
        "s_nop 0\n"
        "s_nop 0\n"
        "s_nop 0\n"
        "s_nop 0\n"
        "s_nop 0\n"
        "s_nop 0\n"
        "s_nop 0\n"
        "s_nop 0\n"
        "s_nop 0\n"
        "s_nop 0\n"
        "s_nop 0\n"
        "s_nop 0\n"
        "s_nop 0\n"
        "s_nop 0\n"
        "Lpzc%=:\n"
        "global_load_dword v39, %[l64], s[48:49] sc1\n"
        "s_nop 15\n"
        "s_nop 15\n"
        "global_store_dwordx4 %[pz], v[60:63], s[44:45] sc1\n"
        "global_store_dwordx4 %[pz], v[60:63], s[44:45] offset:1024 sc1\n"
        "s_mov_b64 exec, 0xffffffff\n"
        "global_store_dword %[mst], v60, s[96:97] sc1\n"
        "s_mov_b64 exec, 1\n"
        "global_store_byte %[rep], v43, s[50:51] sc1\n"
        "s_mov_b64 exec, -1\n"
        "s_add_u32 s48, s48, 0x40000\n"
        "s_addc_u32 s49, s49, 0\n"
        "s_xor_b32 s44, s44, 0x100000\n"
        "s_mov_b32 s96, s52\n"
        "s_add_u32 s54, s54, 1\n"
        "s_cmp_eq_u32 s54, 3\n"
        "s_cselect_b32 s54, 0, s54\n"
        "s_mov_b32 s66, 0x10000\n"
        "s_cselect_b32 s65, 0xfffe0000, s66\n"
        "s_add_u32 s52, s52, s65\n"
        "s_add_u32 s36, s36, 1\n"
        "s_waitcnt vmcnt(5)\n"
        "s_cmp_eq_u32 %[agg], 0\n"
        "s_cbranch_scc1 Lhave%=\n"
        "v_bfe_u32 v46, v41, 8, 8\n"
        "v_bfe_u32 v44, v41, 16, 8\n"
        "v_lshrrev_b32 v45, 24, v41\n"
        "v_and_b32 v41, 0xff, v41\n"
        "v_max3_u32 v41, v41, v46, v44\n"
        "v_max_u32 v41, v41, v45\n"
        "s_nop 1\n"
        "v_max_u32_dpp v41, v41, v41 row_ror:8 row_mask:0xf bank_mask:0xf\n"
        "s_nop 1\n"
        "v_max_u32_dpp v41, v41, v41 row_ror:4 row_mask:0xf bank_mask:0xf\n"
        "s_nop 1\n"
        "v_max_u32_dpp v41, v41, v41 row_ror:2 row_mask:0xf bank_mask:0xf\n"
        "s_nop 1\n"
        "v_max_u32_dpp v41, v41, v41 row_ror:1 row_mask:0xf bank_mask:0xf\n"
        "s_nop 0\n"
        "v_readlane_b32 s65, v41, 0\n"
        "v_readlane_b32 s66, v41, 16\n"
        "s_max_u32 s65, s65, s66\n"
        "s_lshl_b32 s65, s65, 1\n"
        "s_cselect_b32 s65, s65, s92\n"
        "s_sub_u32 s65, s65, s92\n"
        "s_min_i32 s65, s65, 12\n"
        "s_max_i32 s65, s65, -1\n"
        "s_add_u32 s92, s92, s65\n"
        "v_mov_b32 v45, s92\n"
        "v_mov_b32 v44, 4352\n"
        "s_mov_b64 exec, 1\n"
        "global_store_dword v44, v45, %[sb] sc1\n"
        "s_mov_b64 exec, -1\n"
        "s_branch Lhave%=\n"
        "Lmiss%=:\n"
        "s_add_u32 s99, s99, 1\n"
        "s_mov_b32 s93, 1\n"
        "s_nop 6\n"
        "Lagain%=:\n"
        "s_add_u32 s67, s67, 1\n"
        "s_cmp_gt_u32 s67, 262144\n"
        "s_cbranch_scc1 Lfail%=\n"
        "s_sleep 2\n"
        "s_cmp_eq_u32 s36, 1\n"
        "s_cbranch_scc0 Lstep%=\n"
        "s_sleep 5\n"
        "s_branch Lstep%=\n"
        "Lfail%=:\n"
        "s_mov_b32 s99, -1\n"
        "Ldone%=:\n"
        "s_waitcnt vmcnt(0) lgkmcnt(0)\n"
        "s_mov_b32 %[o0], s99\n"
        "s_mov_b32 %[o2], s67\n"
        : [o0] "=&s"(o0), [o2] "=&s"(o2)
        : [rl] "v"(rl), [pz] "v"(pz), [mst] "v"(mst), [stl] "v"(stl), [idx] "v"(idx), [pb] "s"(pb), [mb] "s"(mb), [sb] "s"(sb), [DA] "s"(DA), [DB] "s"(p.DB), [DZ] "s"(p.DZ), [DP] "s"(p.DP), [CJ] "s"(p.GAP - w * p.BQ), [rep] "v"(id * 128 + w), [agg] "s"(agg),
          [kv] "s"(kvp), [l64] "v"(l * 64)
        : "s36", "s37", "s38", "s39", "s40", "s41", "s42", "s43", "s44", "s45", "s46", "s47", "s48", "s49", "s50", "s51", "s52", "s53", "s54", "s55", "s56", "s57", "s58", "s59", "s60", "s61", "s62", "s63", "s64", "s65", "s66", "s67", "s68", "s69", "s70", "s71", "s72", "s73", "s74", "s75", "s76", "s77", "s78", "s79", "s80", "s81", "s82", "s83", "s84", "s85", "s86", "s87", "s88", "s89", "s90", "s91", "s92", "s93", "s94", "s95", "s96", "s97", "s98", "s99", "v39", "v40", "v41", "v42", "v43", "v44", "v45", "v46", "v47", "v48", "v49", "v50", "v51", "v52", "v53", "v54", "v55", "v56", "v57", "v58", "v59", "v60", "v61", "v62", "v63", "vcc", "scc", "memory");
}

__global__ void __launch_bounds__(256, 1) rmt_geglu_kernel(Params p) {
    extern __shared__ __attribute__((aligned(16))) u8 lds[];
    const int b = blockIdx.x, x = b & 7, r = b >> 3;
    const int w = __builtin_amdgcn_readfirstlane(threadIdx.x >> 6), l = threadIdx.x & 63;
    if (r < 4) {
        unsigned long long pc; unsigned len;
        asm volatile("s_getpc_b64 %0\n.Lrmt_pc:\n\ts_mov_b32 %1, .Lrmt_end-.Lrmt_pc" : "=s"(pc), "=s"(len));
        touch((const u8*)pc + min((unsigned)((r * 4 + w) * 4096 + l * 64), len - 4), (x & 5) ? 0x24000 : L_O);
    }
    if (x == 0 || x == 2) {
        const int id = (r & 15) + 8 * x;
        if (r < 16) head_role(p, id, w); else red_role(p, tick_read(p), id, w, l);
        return;
    }
    const int xi = x == 1 ? 0 : x - 2, jq = (r - 16) * 6 + xi, j = r * 3 + xi - 3;
    STAMP(0);
    if (r < 16 || jq >= 64) {
        const unsigned seen = tick_read(p);
        const int tl = r < 16 ? r * 6 + xi : jq + 32;
        if ((3 == 1 || 3 == 3) && (r >= 16 || (xi < 3 && r * 3 + xi >= 16))) { for (int i = 0; i < 3; ++i) __builtin_amdgcn_s_sleep(18); warm(p, r >= 16 ? jq - 64 : r * 3 + xi + 16, w, l); }
        if (b != 127) for (int t = tl; t <= (b == 119 ? 95 : tl); t += 6) ln_tile(p, t != 95, seen, t, w, l);
        else tick_master(p, seen, w, l);
    }
    if (r < 16 && xi >= 3 && j >= 28 && j < 36) poison(p, j - 28, w, l);
    if (3 == 2 && r < 16 && (xi < 3 || j < 16)) warm(p, xi < 3 ? r * 3 + xi : 48 + j, w, l);
    if (r < 16) for (int i = 0; i < 3; ++i) __builtin_amdgcn_s_sleep(36);
    if (r >= 16) qkv_role(jq & 31, ((jq >> 5) + 1) % 3, 1 - (jq >> 6), w);
    const int jf = (r & 15) * 3 + (xi < 3 ? xi : xi - 3);
    if ((r >= 16) != (xi >= 3) && jf < 28) ffn_role(p, jf, r, r < 16 ? 0 : H0, r < 16 ? H0 : 64, w, l);
    if ((r >= 16) == (xi >= 3)) out_role(p, lds, xi, r & 15, w, l);
    else if (r < 16) at_role(p, lds, (j < 28 ? j + 20 : j - 28) * 4 + w, l);
}

extern "C" void kernel_launch(void* const* d_in, const int* in_sizes, int n_in, void* d_out, int out_size, void* d_ws, size_t ws_size, hipStream_t stream) {
    Params p;
    p.x = (const u16*)d_in[0]; p.mem0 = (const u16*)d_in[1]; p.Wq = (const u16*)d_in[2]; p.Wk = (const u16*)d_in[3]; p.Wv = (const u16*)d_in[4]; p.Wo = (const u16*)d_in[5];
    p.wg = (const u16*)d_in[8]; p.wu = (const u16*)d_in[9]; p.wd = (const u16*)d_in[10];
    p.out = (u16*)d_out; p.ws = (u8*)d_ws;
    p.win = (u8*)(((size_t)d_ws + F_BYTES + (2u << 20) - 1) & ~(size_t)((2u << 20) - 1));
    p.DA0 = -86; p.DA1 = -48; p.DA2 = -10; p.DA3 = 27; p.DB = 196; p.DZ = 200; p.DP = 133; p.D0 = 1070; p.GAP = -15; p.BQ = 38;
    (void)hipFuncSetAttribute((const void*)rmt_geglu_kernel, hipFuncAttributeMaxDynamicSharedMemorySize, L_END);
    rmt_geglu_kernel<<<256, 256, L_END, stream>>>(p);
}
```
